```python
import torch

def _softmax(x, axis=-1):
    return torch.softmax(x, dim=axis)

def _gelu(x):
    return torch.nn.functional.gelu(x, approximate='tanh')

def _silu(x):
    return torch.nn.functional.silu(x)

def _sigmoid(x):
    return torch.sigmoid(x)

def _layernorm(x, axis=-1, eps=1e-05):
    m = x.mean(dim=axis, keepdim=True)
    v = x.var(dim=axis, keepdim=True, unbiased=False)
    return (x - m) / torch.sqrt(v + eps)

def _scatter_add(tgt, idx, src, axis=0):
    return tgt.clone().index_add_(axis, idx, src)

def _segment_sum(src, seg, n):
    out = torch.zeros((n,) + src.shape[1:], dtype=src.dtype)
    return out.index_add_(0, seg, src)

def _pad(x, pw):
    flat = tuple((v for pair in reversed(pw) for v in pair))
    return torch.nn.functional.pad(x, flat)
kernel_name = 'standalone__tree_speculative_verify_tree_attention_over_candidate_token_tree__sampling'

def setup_inputs():

    _w = lambda a: (lambda t: t.to(torch.bfloat16) if t.is_floating_point() else t)(torch.as_tensor(a))
    B, Hq, Hkv, T, C, D = (8, 16, 8, 32, 2048, 256)
    tree_mask = torch.zeros((B, T, T), dtype=float)
    for b in range(B):
        parent = torch.zeros(T, dtype=torch.int64)
        for i in range(1, T):
            parent[i] = int(torch.randint(0, i, (1,)).item())
        for i in range(T):
            j = i
            tree_mask[b, i, j] = 1.0
            while j > 0:
                j = parent[j]
                tree_mask[b, i, j] = 1.0
    return {'q': _w((lambda *s: torch.randn(*s, dtype=torch.bfloat16) * 0.8)(B, Hq, T, D)), 'k_cache': _w((lambda *s: torch.randn(*s, dtype=torch.bfloat16) * 0.8)(B, Hkv, C, D)), 'v_cache': _w((lambda *s: torch.randn(*s, dtype=torch.bfloat16) * 0.05)(B, Hkv, C, D)), 'k_tree': _w((lambda *s: torch.randn(*s, dtype=torch.bfloat16) * 0.8)(B, Hkv, T, D)), 'v_tree': _w((lambda *s: torch.randn(*s, dtype=torch.bfloat16) * 0.05)(B, Hkv, T, D)), 'tree_mask': _w(tree_mask)}

def reference(q, k_cache, v_cache, k_tree, v_tree, tree_mask):
    B, Hq, T, D = q.shape
    Hkv, C = (k_cache.shape[1], k_cache.shape[2])
    rep = Hq // Hkv
    kc = (lambda x, n, axis: torch.repeat_interleave(x, n, dim=axis))(k_cache, rep, axis=1)
    vc = (lambda x, n, axis: torch.repeat_interleave(x, n, dim=axis))(v_cache, rep, axis=1)
    kt = (lambda x, n, axis: torch.repeat_interleave(x, n, dim=axis))(k_tree, rep, axis=1)
    vt = (lambda x, n, axis: torch.repeat_interleave(x, n, dim=axis))(v_tree, rep, axis=1)
    k = torch.concatenate([kc, kt], axis=2)
    v = torch.concatenate([vc, vt], axis=2)
    scale = D ** (-0.5)
    scores = torch.einsum('bhqd,bhkd->bhqk', q, k) * scale
    ctx_mask = torch.ones((B, 1, T, C), dtype=scores.dtype)
    tm = tree_mask[:, None, :, :].clone()
    full_mask = torch.concatenate([ctx_mask, tm], axis=-1)
    scores = scores + (1.0 - full_mask) * -1000000000.0
    p = _softmax(scores, axis=-1)
    return torch.einsum('bhqk,bhkd->bhqd', p, v)
```

```cpp
#include <hip/hip_runtime.h>
#include <hip/hip_bf16.h>
#include <stdint.h>

namespace {
constexpr int NB = 8, HQ = 16, HKV = 8, TT = 32, CC = 2048, DD = 256;
constexpr int NSPLIT = 4;
constexpr int NGROUP = NB * HKV;
constexpr int SLOT = 16384;
constexpr int RING = 8;
constexpr int MASK_OFF = RING * SLOT;
constexpr int LDS_BYTES = MASK_OFF + 2048;
constexpr int TILES = CC / NSPLIT / 32;
constexpr float LOG2E = 1.4426950408889634f;
constexpr float SC = 0.0625f * LOG2E;
constexpr float MASKB = -1.0e9f * LOG2E;

typedef __attribute__((ext_vector_type(8))) __bf16 bf16x8;
typedef __attribute__((ext_vector_type(4))) __bf16 bf16x4;
typedef __attribute__((ext_vector_type(2))) __bf16 bf16x2;
typedef __attribute__((ext_vector_type(4))) float f32x4;
typedef __attribute__((ext_vector_type(2))) float f32x2;
typedef __attribute__((ext_vector_type(4))) unsigned u32x4;
typedef __attribute__((ext_vector_type(2))) unsigned u32x2;
#define LAS __attribute__((address_space(3)))
#define DI __device__ __forceinline__

#define KV_NT 1
#define NTS " nt"

DI void dma_slot(const char* sbase, uint32_t m0v, uint32_t v0, uint32_t v1, uint32_t v2, uint32_t v3) {
  uint32_t keep;
  asm volatile(
      "s_mov_b32 %0, m0\n\ts_mov_b32 m0, %6\n\ts_nop 0\n\t"
      "global_load_lds_dwordx4 %1, %5" NTS "\n\t"
      "global_load_lds_dwordx4 %2, %5 offset:1024" NTS "\n\t"
      "global_load_lds_dwordx4 %3, %5 offset:2048" NTS "\n\t"
      "global_load_lds_dwordx4 %4, %5 offset:3072" NTS "\n\t"
      "s_mov_b32 m0, %0"
      : "=&s"(keep) : "v"(v0), "v"(v1), "v"(v2), "v"(v3), "s"(sbase), "s"(m0v) : "memory");
}
DI void dma_one(const char* sbase, uint32_t m0v, uint32_t v0) {
  uint32_t keep;
  asm volatile("s_mov_b32 %0, m0\n\ts_mov_b32 m0, %3\n\ts_nop 0\n\t"
               "global_load_lds_dwordx4 %1, %2\n\t"
               "s_mov_b32 m0, %0"
               : "=&s"(keep) : "v"(v0), "s"(sbase), "s"(m0v) : "memory");
}
template <int N> DI void wait_vm() { asm volatile("s_waitcnt vmcnt(%0)" ::"i"(N) : "memory"); }
DI void wait_slots(int k) {
  if (k >= RING - 2) wait_vm<4 * (RING - 2)>();
  else if (k == 5) wait_vm<20>();
  else if (k == 4) wait_vm<16>();
  else if (k == 3) wait_vm<12>();
  else if (k == 2) wait_vm<8>();
  else if (k == 1) wait_vm<4>();
  else wait_vm<0>();
}
DI void bar() { asm volatile("s_waitcnt lgkmcnt(0)\n\ts_barrier" ::: "memory"); }
DI float xg_max(float x) {
  auto s = __builtin_amdgcn_permlane16_swap(__float_as_uint(x), __float_as_uint(x), false, false);
  x = fmaxf(__uint_as_float(s[0]), __uint_as_float(s[1]));
  auto t = __builtin_amdgcn_permlane32_swap(__float_as_uint(x), __float_as_uint(x), false, false);
  return fmaxf(__uint_as_float(t[0]), __uint_as_float(t[1]));
}
DI float xg_sum(float x) {
  auto s = __builtin_amdgcn_permlane16_swap(__float_as_uint(x), __float_as_uint(x), false, false);
  x = __uint_as_float(s[0]) + __uint_as_float(s[1]);
  auto t = __builtin_amdgcn_permlane32_swap(__float_as_uint(x), __float_as_uint(x), false, false);
  return __uint_as_float(t[0]) + __uint_as_float(t[1]);
}
DI float bf2f(unsigned short x) { return __uint_as_float((unsigned)x << 16); }

__global__ void __launch_bounds__(256, 1)
attn_split(const char* __restrict__ q, const char* __restrict__ kc, const char* __restrict__ vc,
           const char* __restrict__ kt, const char* __restrict__ vt, const char* __restrict__ mask,
           float* __restrict__ po, float* __restrict__ pml) {
  __shared__ __attribute__((aligned(1024))) char smem[LDS_BYTES];
  const int tid = threadIdx.x, lane = tid & 63;
  const int w = __builtin_amdgcn_readfirstlane(tid >> 6);
  const int bid = blockIdx.x;
  const int grp = (bid & 7) * 8 + (bid >> 5), split = (bid >> 3) & 3;
  const int has_tree = (split == NSPLIT - 1) ? 1 : 0;
  const int ntiles = TILES + has_tree;
  const int nslots = 2 + 2 * ntiles;
  const uint32_t lds0 = (uint32_t)(uintptr_t)(LAS char*)smem;

  const int hi = lane >> 5, pos = lane & 31;
  uint32_t kofs[4], vofs[4];
#pragma unroll
  for (int j = 0; j < 4; ++j) {
    const int row = 8 * w + 2 * j + hi;
    kofs[j] = 4096 * w + hi * 512 + ((pos ^ (row & 15)) << 4);
    vofs[j] = 4096 * w + hi * 512 + ((pos ^ ((row & 7) << 1)) << 4);
  }
  const char* qg = q + (size_t)grp * (64 * 512);
  const char* kcg = kc + ((size_t)grp * CC + (size_t)split * (CC / NSPLIT)) * 512 - (size_t)has_tree * SLOT;
  const char* vcg = vc + ((size_t)grp * CC + (size_t)split * (CC / NSPLIT)) * 512 - (size_t)has_tree * SLOT;
  const char* ktg = kt + (size_t)grp * SLOT;
  const char* vtg = vt + (size_t)grp * SLOT;
  const uint32_t m0w = lds0 + 4096 * w;

  auto issueK = [&](int t) {
    const char* src = (has_tree && t == 0) ? ktg : kcg + (size_t)t * SLOT;
    dma_slot(src, m0w + ((2 + 2 * t) & (RING - 1)) * SLOT, kofs[0], kofs[1], kofs[2], kofs[3]);
  };
  auto issueV = [&](int t) {
    const char* src = (has_tree && t == 0) ? vtg : vcg + (size_t)t * SLOT;
    dma_slot(src, m0w + ((3 + 2 * t) & (RING - 1)) * SLOT, vofs[0], vofs[1], vofs[2], vofs[3]);
  };

  if (has_tree && w < 2) dma_one(mask + (size_t)(grp >> 3) * 2048 + w * 1024, lds0 + MASK_OFF + w * 1024, lane * 16);
  dma_slot(qg, m0w, kofs[0], kofs[1], kofs[2], kofs[3]);
  dma_slot(qg + SLOT, m0w + SLOT, kofs[0], kofs[1], kofs[2], kofs[3]);
  issueK(0); issueV(0); issueK(1); issueV(1); issueK(2); issueV(2);

  const int r = lane & 15, g = lane >> 4;
  const uint32_t kra = r * 512 + ((g ^ r) << 4);
  const int vrow = 4 * g + (r >> 2);
  const uint32_t vra = vrow * 512 + ((vrow & 7) << 5) + ((r & 3) << 3);

  wait_vm<24>();
  bar();
  bf16x8 qf[8];
  {
    const uint32_t qb = (w >> 1) * SLOT + (w & 1) * 8192 + kra;
#pragma unroll
    for (int s = 0; s < 8; ++s) qf[s] = *(const LAS bf16x8*)((LAS char*)smem + ((qb ^ (64 * (s & 3))) + 256 * (s >> 2)));
  }
  bar();
  issueK(3);

  f32x4 o[16];
#pragma unroll
  for (int i = 0; i < 16; ++i) o[i] = f32x4{0.f, 0.f, 0.f, 0.f};
  float m = -__builtin_inff(), l = 0.f;

  for (int t = 0; t < ntiles; ++t) {
    const int sk = 2 + 2 * t;
    wait_slots(nslots - 1 - sk);
    bar();
    if (t + 3 < ntiles) issueV(t + 3);
    f32x4 s0 = {0.f, 0.f, 0.f, 0.f}, s1 = {0.f, 0.f, 0.f, 0.f};
    {
      const uint32_t kb = (sk & (RING - 1)) * SLOT + kra;
#pragma unroll
      for (int s = 0; s < 8; ++s) {
        const uint32_t a = (kb ^ (64 * (s & 3))) + 256 * (s >> 2);
        const bf16x8 a0 = *(const LAS bf16x8*)((LAS char*)smem + a);
        const bf16x8 a1 = *(const LAS bf16x8*)((LAS char*)smem + a + 8192);
        s0 = __builtin_amdgcn_mfma_f32_16x16x32_bf16(a0, qf[s], s0, 0, 0, 0);
        s1 = __builtin_amdgcn_mfma_f32_16x16x32_bf16(a1, qf[s], s1, 0, 0, 0);
      }
    }
    float x[8];
#pragma unroll
    for (int j = 0; j < 4; ++j) { x[j] = s0[j] * SC; x[4 + j] = s1[j] * SC; }
    if (has_tree && t == 0) {
      const uint32_t ma = MASK_OFF + (16 * (w & 1) + r) * 64 + 8 * g;
      const u32x2 m0 = *(const LAS u32x2*)((LAS char*)smem + ma);
      const u32x2 m1 = *(const LAS u32x2*)((LAS char*)smem + ma + 32);
      const float mk[8] = {__uint_as_float(m0[0] << 16), __uint_as_float(m0[0] & 0xffff0000u), __uint_as_float(m0[1] << 16), __uint_as_float(m0[1] & 0xffff0000u),
                           __uint_as_float(m1[0] << 16), __uint_as_float(m1[0] & 0xffff0000u), __uint_as_float(m1[1] << 16), __uint_as_float(m1[1] & 0xffff0000u)};
#pragma unroll
      for (int j = 0; j < 8; ++j) x[j] += (1.0f - mk[j]) * MASKB;
    }
    float mx = fmaxf(fmaxf(fmaxf(x[0], x[1]), fmaxf(x[2], x[3])), fmaxf(fmaxf(x[4], x[5]), fmaxf(x[6], x[7])));
    mx = xg_max(mx);
    if (__any(mx > m)) {
      const float mn = fmaxf(m, mx);
      const float al = __builtin_amdgcn_exp2f(m - mn);
      m = mn; l *= al;
#pragma unroll
      for (int i = 0; i < 16; ++i) o[i] *= al;
    }
    float p[8];
#pragma unroll
    for (int j = 0; j < 8; ++j) p[j] = __builtin_amdgcn_exp2f(x[j] - m);
    l += ((p[0] + p[1]) + (p[2] + p[3])) + ((p[4] + p[5]) + (p[6] + p[7]));
    bf16x8 pf;
#pragma unroll
    for (int j = 0; j < 8; ++j) pf[j] = (__bf16)p[j];
    wait_slots(nslots - 2 - sk);
    bar();
    if (t + 4 < ntiles) issueK(t + 4);
    {
      const uint32_t vb = ((sk + 1) & (RING - 1)) * SLOT + vra;
#pragma unroll
      for (int db = 0; db < 16; ++db) {
        const uint32_t a = (vb ^ (32 * (db & 7))) + 256 * (db >> 3);
        const bf16x4 lo = __builtin_amdgcn_ds_read_tr16_b64_v4bf16((LAS bf16x4*)((LAS char*)smem + a));
        const bf16x4 hi4 = __builtin_amdgcn_ds_read_tr16_b64_v4bf16((LAS bf16x4*)((LAS char*)smem + a + 8192));
        const bf16x8 av = __builtin_shufflevector(lo, hi4, 0, 1, 2, 3, 4, 5, 6, 7);
        o[db] = __builtin_amdgcn_mfma_f32_16x16x32_bf16(av, pf, o[db], 0, 0, 0);
      }
    }
  }
  l = xg_sum(l);
  float* pw = po + ((((size_t)grp * NSPLIT + split) * 4 + w) * 16) * 256 + lane * 4;
#pragma unroll
  for (int db = 0; db < 16; ++db) *(f32x4*)(pw + db * 256) = o[db];
  if (g == 0) *(f32x2*)(pml + (((size_t)grp * NSPLIT + split) * 64 + 16 * w + r) * 2) = f32x2{m, l};
}

__global__ void __launch_bounds__(256)
attn_combine(const float* __restrict__ po, const float* __restrict__ pml, unsigned short* __restrict__ out) {
  const int tid = threadIdx.x, lane = tid & 63, w = tid >> 6, r = lane & 15, g = lane >> 4;
  const int bid = blockIdx.x;
  const int grp = (bid & 7) * 8 + (bid >> 5), j = (bid >> 3) & 3;
  float ms[NSPLIT], ls[NSPLIT];
#pragma unroll
  for (int s = 0; s < NSPLIT; ++s) {
    const f32x2 v = *(const f32x2*)(pml + (((size_t)grp * NSPLIT + s) * 64 + 16 * w + r) * 2);
    ms[s] = v[0]; ls[s] = v[1];
  }
  f32x4 a[NSPLIT][4];
#pragma unroll
  for (int s = 0; s < NSPLIT; ++s)
#pragma unroll
    for (int i = 0; i < 4; ++i)
      a[s][i] = *(const f32x4*)(po + ((((size_t)grp * NSPLIT + s) * 4 + w) * 16 + 4 * j + i) * 256 + lane * 4);
  const float M = fmaxf(fmaxf(ms[0], ms[1]), fmaxf(ms[2], ms[3]));
  float L = 0.f, wt[NSPLIT];
#pragma unroll
  for (int s = 0; s < NSPLIT; ++s) { wt[s] = __builtin_amdgcn_exp2f(ms[s] - M); L += wt[s] * ls[s]; }
  const float inv = 1.0f / L;
  unsigned short* orow = out + ((size_t)grp * 64 + 16 * w + r) * DD + 64 * j + 4 * g;
#pragma unroll
  for (int i = 0; i < 4; ++i) {
    f32x4 v = a[0][i] * wt[0] + a[1][i] * wt[1] + a[2][i] * wt[2] + a[3][i] * wt[3];
    v *= inv;
    bf16x4 b = {(__bf16)v[0], (__bf16)v[1], (__bf16)v[2], (__bf16)v[3]};
    *(bf16x4*)(orow + 16 * i) = b;
  }
}
}

extern "C" void kernel_launch(void* const* d_in, const int* in_sizes, int n_in, void* d_out, int out_size, void* d_ws,
                              size_t ws_size, hipStream_t stream) {
  float* po = (float*)d_ws;
  float* pml = po + (size_t)NGROUP * NSPLIT * 64 * DD;
  attn_split<<<NGROUP * NSPLIT, 256, 0, stream>>>((const char*)d_in[0], (const char*)d_in[1], (const char*)d_in[2],
                                                  (const char*)d_in[3], (const char*)d_in[4], (const char*)d_in[5], po, pml);
  attn_combine<<<NGROUP * NSPLIT, 256, 0, stream>>>(po, pml, (unsigned short*)d_out);
}
```

```cpp
#include <hip/hip_runtime.h>
#include <hip/hip_bf16.h>
#include <stdint.h>

namespace {
constexpr int NB = 8, HQ = 16, HKV = 8, TT = 32, CC = 2048, DD = 256;
constexpr int NSPLIT = 4;
constexpr int NGROUP = NB * HKV;
constexpr int SLOT = 16384;
#define RING_N 4
constexpr int RING = RING_N;
constexpr int MASK_OFF = RING * SLOT;
constexpr int LDS_BYTES = MASK_OFF + 2048;
constexpr int TILES = CC / NSPLIT / 32;
constexpr float LOG2E = 1.4426950408889634f;
constexpr float SC = 0.0625f * LOG2E;
constexpr float MASKB = -1.0e9f * LOG2E;
constexpr size_t WS_FLAG = 0;
constexpr size_t WS_ML = 65536;
constexpr size_t WS_PO = WS_ML + (size_t)256 * 64 * 8;
constexpr size_t WS_END = WS_PO + (size_t)256 * 65536;

typedef __attribute__((ext_vector_type(8))) __bf16 bf16x8;
typedef __attribute__((ext_vector_type(4))) __bf16 bf16x4;
typedef __attribute__((ext_vector_type(4))) float f32x4;
typedef __attribute__((ext_vector_type(2))) float f32x2;
typedef __attribute__((ext_vector_type(4))) unsigned u32x4;
typedef __attribute__((ext_vector_type(2))) unsigned u32x2;
#define LAS __attribute__((address_space(3)))
#define GAS __attribute__((address_space(1)))
#define DI __device__ __forceinline__
#define SBAR() __builtin_amdgcn_sched_barrier(0)
#define TS(i)

DI void dma_slot(const char* sbase, uint32_t m0v, uint32_t v0, uint32_t v1, uint32_t v2, uint32_t v3) {
  uint32_t keep;
  asm volatile(
      "s_mov_b32 %0, m0\n\ts_mov_b32 m0, %6\n\ts_nop 0\n\t"
      "global_load_lds_dwordx4 %1, %5 nt\n\t"
      "global_load_lds_dwordx4 %2, %5 offset:1024 nt\n\t"
      "global_load_lds_dwordx4 %3, %5 offset:2048 nt\n\t"
      "global_load_lds_dwordx4 %4, %5 offset:3072 nt\n\t"
      "s_mov_b32 m0, %0"
      : "=&s"(keep) : "v"(v0), "v"(v1), "v"(v2), "v"(v3), "s"(sbase), "s"(m0v) : "memory");
}
DI void dma_one(const char* sbase, uint32_t m0v, uint32_t v0) {
  uint32_t keep;
  asm volatile("s_mov_b32 %0, m0\n\ts_mov_b32 m0, %3\n\ts_nop 0\n\t"
               "global_load_lds_dwordx4 %1, %2\n\t"
               "s_mov_b32 m0, %0"
               : "=&s"(keep) : "v"(v0), "s"(sbase), "s"(m0v) : "memory");
}
template <int N> DI void wait_vm() { asm volatile("s_waitcnt vmcnt(%0)" ::"i"(N) : "memory"); }
DI void wait_slots(int k) {
  if (k >= RING - 2) { wait_vm<4 * (RING - 2)>(); return; }
  if (RING > 7 && k == 5) wait_vm<20>();
  else if (RING > 6 && k == 4) wait_vm<16>();
  else if (RING > 5 && k == 3) wait_vm<12>();
  else if (RING > 4 && k == 2) wait_vm<8>();
  else if (k == 1) wait_vm<4>();
  else wait_vm<0>();
}
DI void bar() { asm volatile("s_waitcnt lgkmcnt(0)\n\ts_barrier" ::: "memory"); }
DI float xg_max(float x) {
  auto s = __builtin_amdgcn_permlane16_swap(__float_as_uint(x), __float_as_uint(x), false, false);
  x = fmaxf(__uint_as_float(s[0]), __uint_as_float(s[1]));
  auto t = __builtin_amdgcn_permlane32_swap(__float_as_uint(x), __float_as_uint(x), false, false);
  return fmaxf(__uint_as_float(t[0]), __uint_as_float(t[1]));
}
DI float xg_sum(float x) {
  auto s = __builtin_amdgcn_permlane16_swap(__float_as_uint(x), __float_as_uint(x), false, false);
  x = __uint_as_float(s[0]) + __uint_as_float(s[1]);
  auto t = __builtin_amdgcn_permlane32_swap(__float_as_uint(x), __float_as_uint(x), false, false);
  return __uint_as_float(t[0]) + __uint_as_float(t[1]);
}
DI unsigned ld_flag(const unsigned* p) { return __hip_atomic_load((const GAS unsigned*)p, __ATOMIC_RELAXED, __HIP_MEMORY_SCOPE_AGENT); }
DI void st_flag(unsigned* p, unsigned v) { __hip_atomic_store((GAS unsigned*)p, v, __ATOMIC_RELAXED, __HIP_MEMORY_SCOPE_AGENT); }

__global__ void __launch_bounds__(256, 2)
attn_tree(const char* __restrict__ q, const char* __restrict__ kc, const char* __restrict__ vc,
          const char* __restrict__ kt, const char* __restrict__ vt, const char* __restrict__ mask,
          char* __restrict__ ws, unsigned short* __restrict__ out) {
  extern __shared__ __attribute__((aligned(1024))) char smem[];
  const int tid = threadIdx.x, lane = tid & 63;
  const int w = __builtin_amdgcn_readfirstlane(tid >> 6);
  const int bid = blockIdx.x;
  TS(0);
  const int grp = (bid & 7) * 8 + (bid >> 5), split = (bid >> 3) & 3;
  const int has_tree = (split == NSPLIT - 1) ? 1 : 0;
  const int ntiles = TILES + has_tree;
  const int nslots = 2 + 2 * ntiles;
  const uint32_t lds0 = (uint32_t)(uintptr_t)(LAS char*)smem;

  unsigned* flags = (unsigned*)(ws + WS_FLAG) + (size_t)grp * NSPLIT * 32;
  unsigned f0 = 0;
  {
    const uint32_t fo = ((lane & 7) >> 1) * 128 + (lane & 1) * 4;
    asm volatile("global_load_dword %0, %1, %2 sc1" : "=v"(f0) : "v"(fo), "s"(flags) : "memory");
  }

  const int hi = lane >> 5, pos = lane & 31;
  uint32_t kofs[4], vofs[4];
#pragma unroll
  for (int j = 0; j < 4; ++j) {
    const int row = 8 * w + 2 * j + hi;
    kofs[j] = 4096 * w + hi * 512 + ((pos ^ (row & 15)) << 4);
    vofs[j] = 4096 * w + hi * 512 + ((pos ^ ((row & 7) << 1)) << 4);
  }
  const char* qg = q + (size_t)grp * (64 * 512);
  const char* kcg = kc + ((size_t)grp * CC + (size_t)split * (CC / NSPLIT)) * 512 - (size_t)has_tree * SLOT;
  const char* vcg = vc + ((size_t)grp * CC + (size_t)split * (CC / NSPLIT)) * 512 - (size_t)has_tree * SLOT;
  const char* ktg = kt + (size_t)grp * SLOT;
  const char* vtg = vt + (size_t)grp * SLOT;
  const uint32_t m0w = lds0 + 4096 * w;

  auto issue = [&](int sl) {
    const int t = (sl - 2) >> 1;
    const bool tree = has_tree && t == 0;
    const uint32_t dst = m0w + (sl & (RING - 1)) * SLOT;
    if (sl & 1) dma_slot(tree ? vtg : vcg + (size_t)t * SLOT, dst, vofs[0], vofs[1], vofs[2], vofs[3]);
    else dma_slot(tree ? ktg : kcg + (size_t)t * SLOT, dst, kofs[0], kofs[1], kofs[2], kofs[3]);
  };

  if (has_tree && w < 2) dma_one(mask + (size_t)(grp >> 3) * 2048 + w * 1024, lds0 + MASK_OFF + w * 1024, lane * 16);
  dma_slot(qg, m0w, kofs[0], kofs[1], kofs[2], kofs[3]);
  dma_slot(qg + SLOT, m0w + SLOT, kofs[0], kofs[1], kofs[2], kofs[3]);
#pragma unroll
  for (int sl = 2; sl < RING; ++sl) issue(sl);

  const int r = lane & 15, g = lane >> 4;
  const uint32_t kra = r * 512 + ((g ^ r) << 4);
  const int vrow = 4 * g + (r >> 2);
  const uint32_t vra = vrow * 512 + ((vrow & 7) << 5) + ((r & 3) << 3);

  wait_vm<4 * (RING - 2)>();
  asm volatile("" : "+v"(f0));
  bar();
  bf16x8 qf[8];
  {
    const uint32_t qb = (w >> 1) * SLOT + (w & 1) * 8192 + kra;
#pragma unroll
    for (int s = 0; s < 8; ++s) qf[s] = *(const LAS bf16x8*)((LAS char*)smem + ((qb ^ (64 * (s & 3))) + 256 * (s >> 2)));
  }
  bar();
  TS(1);
  issue(RING);

  f32x4 o[16];
#pragma unroll
  for (int i = 0; i < 16; ++i) o[i] = f32x4{0.f, 0.f, 0.f, 0.f};
  float m = -__builtin_inff(), l = 0.f;

#pragma unroll 1
  for (int t = 0; t < ntiles; ++t) {
    const int sk = 2 + 2 * t;
    wait_slots(nslots - 1 - sk);
    bar();
    if (sk + RING - 1 < nslots) issue(sk + RING - 1);
    f32x4 s0 = {0.f, 0.f, 0.f, 0.f}, s1 = {0.f, 0.f, 0.f, 0.f};
    {
      const uint32_t kb = (sk & (RING - 1)) * SLOT + kra;
      bf16x8 a0[8], a1[8];
#pragma unroll
      for (int s = 0; s < 8; ++s) {
        const uint32_t a = (kb ^ (64 * (s & 3))) + 256 * (s >> 2);
        a0[s] = *(const LAS bf16x8*)((LAS char*)smem + a);
        a1[s] = *(const LAS bf16x8*)((LAS char*)smem + a + 8192);
      }
      SBAR();
#pragma unroll
      for (int s = 0; s < 8; ++s) {
        s0 = __builtin_amdgcn_mfma_f32_16x16x32_bf16(a0[s], qf[s], s0, 0, 0, 0);
        s1 = __builtin_amdgcn_mfma_f32_16x16x32_bf16(a1[s], qf[s], s1, 0, 0, 0);
      }
    }
    float x[8];
#pragma unroll
    for (int j = 0; j < 4; ++j) { x[j] = s0[j] * SC; x[4 + j] = s1[j] * SC; }
    if (has_tree && t == 0) {
      const uint32_t ma = MASK_OFF + (16 * (w & 1) + r) * 64 + 8 * g;
      const u32x2 m0 = *(const LAS u32x2*)((LAS char*)smem + ma);
      const u32x2 m1 = *(const LAS u32x2*)((LAS char*)smem + ma + 32);
      const float mk[8] = {__uint_as_float(m0[0] << 16), __uint_as_float(m0[0] & 0xffff0000u), __uint_as_float(m0[1] << 16), __uint_as_float(m0[1] & 0xffff0000u),
                           __uint_as_float(m1[0] << 16), __uint_as_float(m1[0] & 0xffff0000u), __uint_as_float(m1[1] << 16), __uint_as_float(m1[1] & 0xffff0000u)};
#pragma unroll
      for (int j = 0; j < 8; ++j) x[j] += (1.0f - mk[j]) * MASKB;
    }
    float mx = fmaxf(fmaxf(fmaxf(x[0], x[1]), fmaxf(x[2], x[3])), fmaxf(fmaxf(x[4], x[5]), fmaxf(x[6], x[7])));
    mx = xg_max(mx);
    if (__any(mx > m)) {
      const float mn = fmaxf(m, mx);
      const float al = __builtin_amdgcn_exp2f(m - mn);
      m = mn; l *= al;
#pragma unroll
      for (int i = 0; i < 16; ++i) o[i] *= al;
    }
    float p[8];
#pragma unroll
    for (int j = 0; j < 8; ++j) p[j] = __builtin_amdgcn_exp2f(x[j] - m);
    l += ((p[0] + p[1]) + (p[2] + p[3])) + ((p[4] + p[5]) + (p[6] + p[7]));
    bf16x8 pf;
#pragma unroll
    for (int j = 0; j < 8; ++j) pf[j] = (__bf16)p[j];
    asm volatile("" : "+v"(pf), "+v"(l));
    wait_slots(nslots - 2 - sk);
    bar();
    if (sk + RING < nslots) issue(sk + RING);
    {
      const uint32_t vb = ((sk + 1) & (RING - 1)) * SLOT + vra;
      bf16x4 lo[16], hi4[16];
#pragma unroll
      for (int db = 0; db < 16; ++db) {
        const uint32_t a = (vb ^ (32 * (db & 7))) + 256 * (db >> 3);
        lo[db] = __builtin_amdgcn_ds_read_tr16_b64_v4bf16((LAS bf16x4*)((LAS char*)smem + a));
        hi4[db] = __builtin_amdgcn_ds_read_tr16_b64_v4bf16((LAS bf16x4*)((LAS char*)smem + a + 8192));
      }
      SBAR();
#pragma unroll
      for (int db = 0; db < 16; ++db) {
        const bf16x8 av = __builtin_shufflevector(lo[db], hi4[db], 0, 1, 2, 3, 4, 5, 6, 7);
        o[db] = __builtin_amdgcn_mfma_f32_16x16x32_bf16(av, pf, o[db], 0, 0, 0);
      }
    }
  }

  TS(2);
  l = xg_sum(l);
  const int wg = grp * NSPLIT + split;
  const __amdgpu_buffer_rsrc_t rs = __builtin_amdgcn_make_buffer_rsrc(ws, 0, (int)WS_END, 0x00020000);
  {
    const uint32_t pb = (uint32_t)WS_PO + (uint32_t)(wg * 4 + w) * 16384u + lane * 16;
#pragma unroll
    for (int db = 0; db < 16; ++db)
      if ((db >> 2) != split) __builtin_amdgcn_raw_buffer_store_b128(__builtin_bit_cast(u32x4, o[db]), rs, pb + db * 1024, 0, 16);
    if (g == 0) __builtin_amdgcn_raw_buffer_store_b64(u32x2{__float_as_uint(m), __float_as_uint(l)}, rs, (uint32_t)WS_ML + (uint32_t)(wg * 64 + 16 * w + r) * 8u, 0, 16);
  }
  asm volatile("s_waitcnt vmcnt(0)" ::: "memory");
  bar();
  TS(3);
  unsigned beat = __builtin_amdgcn_readlane(f0, 2 * split);
  if (w == 0) {
    const bool mine = ((lane & 7) >> 1) == split;
    const unsigned* fp = flags + ((lane & 7) >> 1) * 32 + (lane & 1);
    if (lane == 0) st_flag(flags + split * 32, ++beat);
    else ++beat;
    for (unsigned it = 1;; ++it) {
      const unsigned v = ld_flag(fp);
      if (__all(mine || (lane & 1) || v != f0)) break;
      if ((it & 63) == 0) { ++beat; if (lane == 0) st_flag(flags + split * 32, beat); }
      __builtin_amdgcn_s_sleep(1);
    }
    if (lane == 0) st_flag(flags + split * 32 + 1, __builtin_amdgcn_readlane(f0, 2 * split + 1) + 1u);
  }
  bar();
  TS(4);
  {
    f32x4 a[3][4]; u32x2 st[3];
#pragma unroll
    for (int k = 0; k < 3; ++k) {
      const int sp = (split + 1 + k) & 3, wo = grp * NSPLIT + sp;
      st[k] = __builtin_amdgcn_raw_buffer_load_b64(rs, (uint32_t)WS_ML + (uint32_t)(wo * 64 + 16 * w + r) * 8u, 0, 16);
      const uint32_t pb = (uint32_t)WS_PO + (uint32_t)(wo * 4 + w) * 16384u + (uint32_t)split * 4096u + lane * 16;
#pragma unroll
      for (int i = 0; i < 4; ++i) a[k][i] = __builtin_bit_cast(f32x4, __builtin_amdgcn_raw_buffer_load_b128(rs, pb + i * 1024, 0, 16));
    }
    f32x4 own[4];
#pragma unroll
    for (int i = 0; i < 4; ++i) own[i] = split == 0 ? o[i] : split == 1 ? o[4 + i] : split == 2 ? o[8 + i] : o[12 + i];
    float M = m;
#pragma unroll
    for (int k = 0; k < 3; ++k) M = fmaxf(M, __uint_as_float(st[k][0]));
    const float w0 = __builtin_amdgcn_exp2f(m - M);
    float L = w0 * l, wt[3];
#pragma unroll
    for (int k = 0; k < 3; ++k) { wt[k] = __builtin_amdgcn_exp2f(__uint_as_float(st[k][0]) - M); L += wt[k] * __uint_as_float(st[k][1]); }
    const float inv = 1.0f / L;
    unsigned short* orow = out + ((size_t)grp * 64 + 16 * w + r) * DD + 64 * split + 4 * g;
#pragma unroll
    for (int i = 0; i < 4; ++i) {
      f32x4 v = own[i] * w0 + a[0][i] * wt[0] + a[1][i] * wt[1] + a[2][i] * wt[2];
      v *= inv;
      bf16x4 b = {(__bf16)v[0], (__bf16)v[1], (__bf16)v[2], (__bf16)v[3]};
      *(bf16x4*)(orow + 16 * i) = b;
    }
  }
  TS(5);
  if (w == 0) {
    const bool mine = ((lane & 7) >> 1) == split;
    const unsigned* fp = flags + ((lane & 7) >> 1) * 32 + (lane & 1);
    for (unsigned it = 1;; ++it) {
      const unsigned v = ld_flag(fp);
      if (__all(mine || !(lane & 1) || v != f0)) break;
      if ((it & 63) == 0) { ++beat; if (lane == 0) st_flag(flags + split * 32, beat); }
      __builtin_amdgcn_s_sleep(1);
    }
  }
}
}

extern "C" void kernel_launch(void* const* d_in, const int* in_sizes, int n_in, void* d_out, int out_size, void* d_ws,
                              size_t ws_size, hipStream_t stream) {
  (void)hipFuncSetAttribute((const void*)attn_tree, hipFuncAttributeMaxDynamicSharedMemorySize, LDS_BYTES);
  attn_tree<<<NGROUP * NSPLIT, 256, LDS_BYTES, stream>>>((const char*)d_in[0], (const char*)d_in[1], (const char*)d_in[2],
                                                 (const char*)d_in[3], (const char*)d_in[4], (const char*)d_in[5],
                                                 (char*)d_ws, (unsigned short*)d_out);
}
```

```cpp
#include <hip/hip_runtime.h>
#include <hip/hip_bf16.h>
#include <stdint.h>
#include <cstdio>
#include <vector>
#include <algorithm>
#define TSTAMP 1

namespace {
constexpr int NB = 8, HQ = 16, HKV = 8, TT = 32, CC = 2048, DD = 256;
constexpr int NSPLIT = 4;
constexpr int NGROUP = NB * HKV;
constexpr int SLOT = 16384;
#define RING_N 4
constexpr int RING = RING_N;
constexpr int MASK_OFF = RING * SLOT;
constexpr int LDS_BYTES = MASK_OFF + 2048;
constexpr int TILES = CC / NSPLIT / 32;
constexpr float LOG2E = 1.4426950408889634f;
constexpr float SC = 0.0625f * LOG2E;
constexpr float MASKB = -1.0e9f * LOG2E;
constexpr size_t WS_FLAG = 0;
constexpr size_t WS_ML = 65536;
constexpr size_t WS_PO = WS_ML + (size_t)256 * 64 * 8;
constexpr size_t WS_END = WS_PO + (size_t)256 * 65536;

typedef __attribute__((ext_vector_type(8))) __bf16 bf16x8;
typedef __attribute__((ext_vector_type(4))) __bf16 bf16x4;
typedef __attribute__((ext_vector_type(4))) float f32x4;
typedef __attribute__((ext_vector_type(2))) float f32x2;
typedef __attribute__((ext_vector_type(4))) unsigned u32x4;
typedef __attribute__((ext_vector_type(2))) unsigned u32x2;
#define LAS __attribute__((address_space(3)))
#define GAS __attribute__((address_space(1)))
#define DI __device__ __forceinline__
#define SBAR() __builtin_amdgcn_sched_barrier(0)
#define TS(i) do { if (w == 0) ts_[i] = __builtin_amdgcn_s_memrealtime(); } while (0)

DI void dma_slot(const char* sbase, uint32_t m0v, uint32_t v0, uint32_t v1, uint32_t v2, uint32_t v3) {
  uint32_t keep;
  asm volatile(
      "s_mov_b32 %0, m0\n\ts_mov_b32 m0, %6\n\ts_nop 0\n\t"
      "global_load_lds_dwordx4 %1, %5 nt\n\t"
      "global_load_lds_dwordx4 %2, %5 offset:1024 nt\n\t"
      "global_load_lds_dwordx4 %3, %5 offset:2048 nt\n\t"
      "global_load_lds_dwordx4 %4, %5 offset:3072 nt\n\t"
      "s_mov_b32 m0, %0"
      : "=&s"(keep) : "v"(v0), "v"(v1), "v"(v2), "v"(v3), "s"(sbase), "s"(m0v) : "memory");
}
DI void dma_one(const char* sbase, uint32_t m0v, uint32_t v0) {
  uint32_t keep;
  asm volatile("s_mov_b32 %0, m0\n\ts_mov_b32 m0, %3\n\ts_nop 0\n\t"
               "global_load_lds_dwordx4 %1, %2\n\t"
               "s_mov_b32 m0, %0"
               : "=&s"(keep) : "v"(v0), "s"(sbase), "s"(m0v) : "memory");
}
template <int N> DI void wait_vm() { asm volatile("s_waitcnt vmcnt(%0)" ::"i"(N) : "memory"); }
DI void wait_slots(int k) {
  if (k >= RING - 2) { wait_vm<4 * (RING - 2)>(); return; }
  if (RING > 7 && k == 5) wait_vm<20>();
  else if (RING > 6 && k == 4) wait_vm<16>();
  else if (RING > 5 && k == 3) wait_vm<12>();
  else if (RING > 4 && k == 2) wait_vm<8>();
  else if (k == 1) wait_vm<4>();
  else wait_vm<0>();
}
DI void bar() { asm volatile("s_waitcnt lgkmcnt(0)\n\ts_barrier" ::: "memory"); }
DI float xg_max(float x) {
  auto s = __builtin_amdgcn_permlane16_swap(__float_as_uint(x), __float_as_uint(x), false, false);
  x = fmaxf(__uint_as_float(s[0]), __uint_as_float(s[1]));
  auto t = __builtin_amdgcn_permlane32_swap(__float_as_uint(x), __float_as_uint(x), false, false);
  return fmaxf(__uint_as_float(t[0]), __uint_as_float(t[1]));
}
DI float xg_sum(float x) {
  auto s = __builtin_amdgcn_permlane16_swap(__float_as_uint(x), __float_as_uint(x), false, false);
  x = __uint_as_float(s[0]) + __uint_as_float(s[1]);
  auto t = __builtin_amdgcn_permlane32_swap(__float_as_uint(x), __float_as_uint(x), false, false);
  return __uint_as_float(t[0]) + __uint_as_float(t[1]);
}
DI unsigned ld_flag(const unsigned* p) { return __hip_atomic_load((const GAS unsigned*)p, __ATOMIC_RELAXED, __HIP_MEMORY_SCOPE_AGENT); }
DI void st_flag(unsigned* p, unsigned v) { __hip_atomic_store((GAS unsigned*)p, v, __ATOMIC_RELAXED, __HIP_MEMORY_SCOPE_AGENT); }

__global__ void __launch_bounds__(256, 2)
attn_tree(const char* __restrict__ q, const char* __restrict__ kc, const char* __restrict__ vc,
          const char* __restrict__ kt, const char* __restrict__ vt, const char* __restrict__ mask,
          char* __restrict__ ws, unsigned short* __restrict__ out) {
  extern __shared__ __attribute__((aligned(1024))) char smem[];
  const int tid = threadIdx.x, lane = tid & 63;
  const int w = __builtin_amdgcn_readfirstlane(tid >> 6);
  const int bid = blockIdx.x;
  unsigned long long ts_[8] = {0, 0, 0, 0, 0, 0, 0, 0};
  TS(0);
  const int grp = (bid & 7) * 8 + (bid >> 5), split = (bid >> 3) & 3;
  const int has_tree = (split == NSPLIT - 1) ? 1 : 0;
  const int ntiles = TILES + has_tree;
  const int nslots = 2 + 2 * ntiles;
  const uint32_t lds0 = (uint32_t)(uintptr_t)(LAS char*)smem;

  unsigned* flags = (unsigned*)(ws + WS_FLAG) + (size_t)grp * NSPLIT * 32;
  unsigned f0 = 0;
  {
    const uint32_t fo = ((lane & 7) >> 1) * 128 + (lane & 1) * 4;
    asm volatile("global_load_dword %0, %1, %2 sc1" : "=v"(f0) : "v"(fo), "s"(flags) : "memory");
  }

  const int hi = lane >> 5, pos = lane & 31;
  uint32_t kofs[4], vofs[4];
#pragma unroll
  for (int j = 0; j < 4; ++j) {
    const int row = 8 * w + 2 * j + hi;
    kofs[j] = 4096 * w + hi * 512 + ((pos ^ (row & 15)) << 4);
    vofs[j] = 4096 * w + hi * 512 + ((pos ^ ((row & 7) << 1)) << 4);
  }
  const char* qg = q + (size_t)grp * (64 * 512);
  const char* kcg = kc + ((size_t)grp * CC + (size_t)split * (CC / NSPLIT)) * 512 - (size_t)has_tree * SLOT;
  const char* vcg = vc + ((size_t)grp * CC + (size_t)split * (CC / NSPLIT)) * 512 - (size_t)has_tree * SLOT;
  const char* ktg = kt + (size_t)grp * SLOT;
  const char* vtg = vt + (size_t)grp * SLOT;
  const uint32_t m0w = lds0 + 4096 * w;

  auto issue = [&](int sl) {
    const int t = (sl - 2) >> 1;
    const bool tree = has_tree && t == 0;
    const uint32_t dst = m0w + (sl & (RING - 1)) * SLOT;
    if (sl & 1) dma_slot(tree ? vtg : vcg + (size_t)t * SLOT, dst, vofs[0], vofs[1], vofs[2], vofs[3]);
    else dma_slot(tree ? ktg : kcg + (size_t)t * SLOT, dst, kofs[0], kofs[1], kofs[2], kofs[3]);
  };

  if (has_tree && w < 2) dma_one(mask + (size_t)(grp >> 3) * 2048 + w * 1024, lds0 + MASK_OFF + w * 1024, lane * 16);
  dma_slot(qg, m0w, kofs[0], kofs[1], kofs[2], kofs[3]);
  dma_slot(qg + SLOT, m0w + SLOT, kofs[0], kofs[1], kofs[2], kofs[3]);
#pragma unroll
  for (int sl = 2; sl < RING; ++sl) issue(sl);

  const int r = lane & 15, g = lane >> 4;
  const uint32_t kra = r * 512 + ((g ^ r) << 4);
  const int vrow = 4 * g + (r >> 2);
  const uint32_t vra = vrow * 512 + ((vrow & 7) << 5) + ((r & 3) << 3);

  wait_vm<4 * (RING - 2)>();
  asm volatile("" : "+v"(f0));
  bar();
  bf16x8 qf[8];
  {
    const uint32_t qb = (w >> 1) * SLOT + (w & 1) * 8192 + kra;
#pragma unroll
    for (int s = 0; s < 8; ++s) qf[s] = *(const LAS bf16x8*)((LAS char*)smem + ((qb ^ (64 * (s & 3))) + 256 * (s >> 2)));
  }
  bar();
  TS(1);
  issue(RING);

  f32x4 o[16];
#pragma unroll
  for (int i = 0; i < 16; ++i) o[i] = f32x4{0.f, 0.f, 0.f, 0.f};
  float m = -__builtin_inff(), l = 0.f;

#pragma unroll 1
  for (int t = 0; t < ntiles; ++t) {
    const int sk = 2 + 2 * t;
    wait_slots(nslots - 1 - sk);
    bar();
    if (sk + RING - 1 < nslots) issue(sk + RING - 1);
    f32x4 s0 = {0.f, 0.f, 0.f, 0.f}, s1 = {0.f, 0.f, 0.f, 0.f};
    {
      const uint32_t kb = (sk & (RING - 1)) * SLOT + kra;
      bf16x8 a0[8], a1[8];
#pragma unroll
      for (int s = 0; s < 8; ++s) {
        const uint32_t a = (kb ^ (64 * (s & 3))) + 256 * (s >> 2);
        a0[s] = *(const LAS bf16x8*)((LAS char*)smem + a);
        a1[s] = *(const LAS bf16x8*)((LAS char*)smem + a + 8192);
      }
      SBAR();
#pragma unroll
      for (int s = 0; s < 8; ++s) {
        s0 = __builtin_amdgcn_mfma_f32_16x16x32_bf16(a0[s], qf[s], s0, 0, 0, 0);
        s1 = __builtin_amdgcn_mfma_f32_16x16x32_bf16(a1[s], qf[s], s1, 0, 0, 0);
      }
    }
    float x[8];
#pragma unroll
    for (int j = 0; j < 4; ++j) { x[j] = s0[j] * SC; x[4 + j] = s1[j] * SC; }
    if (has_tree && t == 0) {
      const uint32_t ma = MASK_OFF + (16 * (w & 1) + r) * 64 + 8 * g;
      const u32x2 m0 = *(const LAS u32x2*)((LAS char*)smem + ma);
      const u32x2 m1 = *(const LAS u32x2*)((LAS char*)smem + ma + 32);
      const float mk[8] = {__uint_as_float(m0[0] << 16), __uint_as_float(m0[0] & 0xffff0000u), __uint_as_float(m0[1] << 16), __uint_as_float(m0[1] & 0xffff0000u),
                           __uint_as_float(m1[0] << 16), __uint_as_float(m1[0] & 0xffff0000u), __uint_as_float(m1[1] << 16), __uint_as_float(m1[1] & 0xffff0000u)};
#pragma unroll
      for (int j = 0; j < 8; ++j) x[j] += (1.0f - mk[j]) * MASKB;
    }
    float mx = fmaxf(fmaxf(fmaxf(x[0], x[1]), fmaxf(x[2], x[3])), fmaxf(fmaxf(x[4], x[5]), fmaxf(x[6], x[7])));
    mx = xg_max(mx);
    if (__any(mx > m)) {
      const float mn = fmaxf(m, mx);
      const float al = __builtin_amdgcn_exp2f(m - mn);
      m = mn; l *= al;
#pragma unroll
      for (int i = 0; i < 16; ++i) o[i] *= al;
    }
    float p[8];
#pragma unroll
    for (int j = 0; j < 8; ++j) p[j] = __builtin_amdgcn_exp2f(x[j] - m);
    l += ((p[0] + p[1]) + (p[2] + p[3])) + ((p[4] + p[5]) + (p[6] + p[7]));
    bf16x8 pf;
#pragma unroll
    for (int j = 0; j < 8; ++j) pf[j] = (__bf16)p[j];
    asm volatile("" : "+v"(pf), "+v"(l));
    wait_slots(nslots - 2 - sk);
    bar();
    if (sk + RING < nslots) issue(sk + RING);
    {
      const uint32_t vb = ((sk + 1) & (RING - 1)) * SLOT + vra;
      bf16x4 lo[16], hi4[16];
#pragma unroll
      for (int db = 0; db < 16; ++db) {
        const uint32_t a = (vb ^ (32 * (db & 7))) + 256 * (db >> 3);
        lo[db] = __builtin_amdgcn_ds_read_tr16_b64_v4bf16((LAS bf16x4*)((LAS char*)smem + a));
        hi4[db] = __builtin_amdgcn_ds_read_tr16_b64_v4bf16((LAS bf16x4*)((LAS char*)smem + a + 8192));
      }
      SBAR();
#pragma unroll
      for (int db = 0; db < 16; ++db) {
        const bf16x8 av = __builtin_shufflevector(lo[db], hi4[db], 0, 1, 2, 3, 4, 5, 6, 7);
        o[db] = __builtin_amdgcn_mfma_f32_16x16x32_bf16(av, pf, o[db], 0, 0, 0);
      }
    }
  }

  TS(2);
  l = xg_sum(l);
  const int wg = grp * NSPLIT + split;
  const __amdgpu_buffer_rsrc_t rs = __builtin_amdgcn_make_buffer_rsrc(ws, 0, (int)WS_END, 0x00020000);
  {
    const uint32_t pb = (uint32_t)WS_PO + (uint32_t)(wg * 4 + w) * 16384u + lane * 16;
#pragma unroll
    for (int db = 0; db < 16; ++db)
      if ((db >> 2) != split) __builtin_amdgcn_raw_buffer_store_b128(__builtin_bit_cast(u32x4, o[db]), rs, pb + db * 1024, 0, 16);
    if (g == 0) __builtin_amdgcn_raw_buffer_store_b64(u32x2{__float_as_uint(m), __float_as_uint(l)}, rs, (uint32_t)WS_ML + (uint32_t)(wg * 64 + 16 * w + r) * 8u, 0, 16);
  }
  asm volatile("s_waitcnt vmcnt(0)" ::: "memory");
  bar();
  TS(3);
  unsigned beat = __builtin_amdgcn_readlane(f0, 2 * split);
  if (w == 0) {
    const bool mine = ((lane & 7) >> 1) == split;
    const unsigned* fp = flags + ((lane & 7) >> 1) * 32 + (lane & 1);
    if (lane == 0) st_flag(flags + split * 32, ++beat);
    else ++beat;
    for (unsigned it = 1;; ++it) {
      const unsigned v = ld_flag(fp);
      if (__all(mine || (lane & 1) || v != f0)) break;
      if ((it & 63) == 0) { ++beat; if (lane == 0) st_flag(flags + split * 32, beat); }
      __builtin_amdgcn_s_sleep(1);
    }
    if (lane == 0) st_flag(flags + split * 32 + 1, __builtin_amdgcn_readlane(f0, 2 * split + 1) + 1u);
  }
  bar();
  TS(4);
  {
    f32x4 a[3][4]; u32x2 st[3];
#pragma unroll
    for (int k = 0; k < 3; ++k) {
      const int sp = (split + 1 + k) & 3, wo = grp * NSPLIT + sp;
      st[k] = __builtin_amdgcn_raw_buffer_load_b64(rs, (uint32_t)WS_ML + (uint32_t)(wo * 64 + 16 * w + r) * 8u, 0, 16);
      const uint32_t pb = (uint32_t)WS_PO + (uint32_t)(wo * 4 + w) * 16384u + (uint32_t)split * 4096u + lane * 16;
#pragma unroll
      for (int i = 0; i < 4; ++i) a[k][i] = __builtin_bit_cast(f32x4, __builtin_amdgcn_raw_buffer_load_b128(rs, pb + i * 1024, 0, 16));
    }
    f32x4 own[4];
#pragma unroll
    for (int i = 0; i < 4; ++i) own[i] = split == 0 ? o[i] : split == 1 ? o[4 + i] : split == 2 ? o[8 + i] : o[12 + i];
    float M = m;
#pragma unroll
    for (int k = 0; k < 3; ++k) M = fmaxf(M, __uint_as_float(st[k][0]));
    const float w0 = __builtin_amdgcn_exp2f(m - M);
    float L = w0 * l, wt[3];
#pragma unroll
    for (int k = 0; k < 3; ++k) { wt[k] = __builtin_amdgcn_exp2f(__uint_as_float(st[k][0]) - M); L += wt[k] * __uint_as_float(st[k][1]); }
    const float inv = 1.0f / L;
    unsigned short* orow = out + ((size_t)grp * 64 + 16 * w + r) * DD + 64 * split + 4 * g;
#pragma unroll
    for (int i = 0; i < 4; ++i) {
      f32x4 v = own[i] * w0 + a[0][i] * wt[0] + a[1][i] * wt[1] + a[2][i] * wt[2];
      v *= inv;
      bf16x4 b = {(__bf16)v[0], (__bf16)v[1], (__bf16)v[2], (__bf16)v[3]};
      *(bf16x4*)(orow + 16 * i) = b;
    }
  }
  TS(5);
  if (w == 0) {
    const bool mine = ((lane & 7) >> 1) == split;
    const unsigned* fp = flags + ((lane & 7) >> 1) * 32 + (lane & 1);
    for (unsigned it = 1;; ++it) {
      const unsigned v = ld_flag(fp);
      if (__all(mine || !(lane & 1) || v != f0)) break;
      if ((it & 63) == 0) { ++beat; if (lane == 0) st_flag(flags + split * 32, beat); }
      __builtin_amdgcn_s_sleep(1);
    }
  }
  TS(6);
  if (tid == 0) { unsigned long long* tp = (unsigned long long*)(ws + (64u << 20)) + bid * 8; for (int i = 0; i < 8; ++i) tp[i] = ts_[i]; }
}
}

__global__ void fill_k(unsigned* p, size_t n, unsigned v) { size_t i = (size_t)blockIdx.x * blockDim.x + threadIdx.x; size_t st = (size_t)gridDim.x * blockDim.x; for (; i < n; i += st) p[i] = v * 2654435761u + (unsigned)i; }
extern "C" void kernel_launch(void* const* d_in, const int* in_sizes, int n_in, void* d_out, int out_size, void* d_ws,
                              size_t ws_size, hipStream_t stream) {
  (void)hipFuncSetAttribute((const void*)attn_tree, hipFuncAttributeMaxDynamicSharedMemorySize, LDS_BYTES);
  auto go = [&]() {
  attn_tree<<<NGROUP * NSPLIT, 256, LDS_BYTES, stream>>>((const char*)d_in[0], (const char*)d_in[1], (const char*)d_in[2],
                                                 (const char*)d_in[3], (const char*)d_in[4], (const char*)d_in[5],
                                                 (char*)d_ws, (unsigned short*)d_out); };
  hipStreamCaptureStatus cs = hipStreamCaptureStatusNone;
  (void)hipStreamIsCapturing(stream, &cs);
  for (int i = 0; i < 6; ++i) fprintf(stderr, "in%d %p ", i, d_in[i]);
  fprintf(stderr, "out %p ws %p capturing %d\n", d_out, d_ws, (int)cs);
  if (cs == hipStreamCaptureStatusNone) {
    void* ptrs[8] = {d_in[0], d_in[1], d_in[2], d_in[3], d_in[4], d_in[5], d_out, d_ws};
    for (int i = 0; i < 8; ++i) {
      hipPointerAttribute_t a; hipError_t e = hipPointerGetAttributes(&a, ptrs[i]);
      void* base = nullptr; size_t sz = 0; hipError_t e2 = hipMemGetAddressRange((hipDeviceptr_t*)&base, &sz, ptrs[i]);
      fprintf(stderr, "ptr%d %p err %d type %d dev %d managed %d flags %u | range err %d base %p size %zu\n", i, ptrs[i], (int)e, (int)a.type, a.device, a.isManaged, a.allocationFlags, (int)e2, base, sz);
    }
    hipEvent_t e0, e1; (void)hipEventCreate(&e0); (void)hipEventCreate(&e1);
    for (int mode = 0; mode < 2; ++mode)
    for (int it = 0; it < 5; ++it) {
      if (mode == 0) fill_k<<<2048, 256, 0, stream>>>((unsigned*)d_ws, ws_size / 4, it);
      (void)hipStreamSynchronize(stream);
      (void)hipEventRecord(e0, stream); go(); (void)hipEventRecord(e1, stream); (void)hipEventSynchronize(e1);
      float ms; (void)hipEventElapsedTime(&ms, e0, e1);
      std::vector<unsigned long long> ts(256 * 8);
      (void)hipMemcpy(ts.data(), (char*)d_ws + (64u << 20), 256 * 64, hipMemcpyDeviceToHost);
      unsigned long long mn = ~0ull; for (int i = 0; i < 256; ++i) mn = std::min(mn, ts[8 * i]);
      fprintf(stderr, "mode %d stamp avg/max:", mode);
      for (int k = 0; k < 7; ++k) { double av = 0, mx = 0; for (int i = 0; i < 256; ++i) { double v = (ts[8 * i + k] - mn) * 0.01; av += v; mx = std::max(mx, v); } fprintf(stderr, " t%d %.2f/%.2f", k, av / 256, mx); }
      fprintf(stderr, "  (event %.2f)\n", ms * 1e3f);
    }
  }
  go();
}
```

```cpp
#include <hip/hip_runtime.h>
#include <hip/hip_bf16.h>
#include <stdint.h>

#define X_RING
namespace {
constexpr int NB = 8, HKV = 8, CC = 2048, DD = 256;
constexpr int NSPLIT = 4;
constexpr int NGROUP = NB * HKV;
constexpr int SLOT = 16384;
constexpr int P_OFF = 4 * SLOT;
constexpr int PW = 17 * 1024;
constexpr int TK_OFF = P_OFF + 4 * PW;
constexpr int TV_OFF = TK_OFF + 4096;
constexpr int PL_OFF = TV_OFF + 4096;
constexpr int LDS_BYTES = PL_OFF + 8192;
constexpr float LOG2E = 1.4426950408889634f;
constexpr float SC = 0.0625f * LOG2E;
constexpr float MASKR = -1.0e9f * 16.0f;
constexpr float SLACK = 10.0f;
constexpr size_t WS_FLAG = 0;
constexpr size_t WS_ML = 65536;
constexpr size_t WS_PO = WS_ML + (size_t)256 * 64 * 8;
constexpr size_t WS_END = WS_PO + (size_t)256 * 32768;

typedef __attribute__((ext_vector_type(8))) __bf16 bf16x8;
typedef __attribute__((ext_vector_type(4))) __bf16 bf16x4;
typedef __attribute__((ext_vector_type(4))) short s16x4;
typedef __attribute__((ext_vector_type(8))) unsigned short u16x8;
typedef __attribute__((ext_vector_type(4))) float f32x4;
typedef __attribute__((ext_vector_type(4))) unsigned u32x4;
typedef __attribute__((ext_vector_type(2))) unsigned u32x2;
#define LAS __attribute__((address_space(3)))
#define GAS __attribute__((address_space(1)))
#define DI __device__ __forceinline__
#define SBAR() __builtin_amdgcn_sched_barrier(0)
#define SPOL 16
#define PST(v, a, s) __builtin_amdgcn_raw_buffer_store_b128(v, rs, a, s, SPOL)
#define PIN(x) asm volatile("" : "+v"(x))
#define TS(i)

DI void dma4(const char* sbase, uint32_t m0v, uint32_t v0, uint32_t v1, uint32_t v2, uint32_t v3) {
  uint32_t keep;
  asm volatile(
      "s_mov_b32 %0, m0\n\ts_mov_b32 m0, %6\n\ts_nop 0\n\t"
      "global_load_lds_dwordx4 %1, %5 nt\n\t"
      "global_load_lds_dwordx4 %2, %5 offset:1024 nt\n\t"
      "global_load_lds_dwordx4 %3, %5 offset:2048 nt\n\t"
      "global_load_lds_dwordx4 %4, %5 offset:3072 nt\n\t"
      "s_mov_b32 m0, %0"
      : "=&s"(keep) : "v"(v0), "v"(v1), "v"(v2), "v"(v3), "s"(sbase), "s"(m0v) : "memory");
}
DI void poll_issue(const void* base, uint32_t off, uint32_t m0v) {
  uint32_t keep;
  asm volatile("s_mov_b32 %0, m0\n\ts_mov_b32 m0, %3\n\ts_nop 0\n\t"
               "global_load_lds_dword %1, %2 sc1\n\t"
               "s_mov_b32 m0, %0"
               : "=&s"(keep) : "v"(off), "s"(base), "s"(m0v) : "memory");
}
template <int N> DI void wait_vm() { asm volatile("s_waitcnt vmcnt(%0)" ::"i"(N) : "memory"); }
DI void bar() { asm volatile("s_waitcnt lgkmcnt(0)\n\ts_barrier" ::: "memory"); }
DI float vmax(float a, float b) { return __builtin_elementwise_maximum(a, b); }
DI float xg_max(float x) {
  auto s = __builtin_amdgcn_permlane16_swap(__float_as_uint(x), __float_as_uint(x), false, false);
  x = vmax(__uint_as_float(s[0]), __uint_as_float(s[1]));
  auto t = __builtin_amdgcn_permlane32_swap(__float_as_uint(x), __float_as_uint(x), false, false);
  return vmax(__uint_as_float(t[0]), __uint_as_float(t[1]));
}
DI unsigned ld_flag(const unsigned* p) { return __hip_atomic_load((const GAS unsigned*)p, __ATOMIC_RELAXED, __HIP_MEMORY_SCOPE_AGENT); }
DI void st_flag(unsigned* p, unsigned v) { __hip_atomic_store((GAS unsigned*)p, v, __ATOMIC_RELAXED, __HIP_MEMORY_SCOPE_AGENT); }
DI unsigned pk2(float a, float b) { typedef __attribute__((ext_vector_type(2))) __bf16 bf16x2; const bf16x2 v = {(__bf16)a, (__bf16)b}; return __builtin_bit_cast(unsigned, v); }

__global__ void __launch_bounds__(384)
attn_tree(const char* __restrict__ q, const char* __restrict__ kc, const char* __restrict__ vc,
          const char* __restrict__ kt, const char* __restrict__ vt, const char* __restrict__ mask,
          char* __restrict__ ws, unsigned short* __restrict__ out) {
  extern __shared__ __attribute__((aligned(1024))) char smem[];
  const int tid = threadIdx.x, lane = tid & 63;
  const int w = __builtin_amdgcn_readfirstlane(tid >> 6);
  const int bid = blockIdx.x;
  TS(0);
  const int grp = (bid & 7) * 8 + (bid >> 5), split = (bid >> 3) & 3;
  const uint32_t lds0 = (uint32_t)(uintptr_t)(LAS char*)smem;
  const int hi = lane >> 5, pos = lane & 31;
  uint32_t ko[8];
#pragma unroll
  for (int j = 0; j < 8; ++j) ko[j] = (j >> 2) * 4096 + hi * 512 + ((pos ^ (2 * j + hi)) << 4);
  const char* kb = kc + ((size_t)grp * CC + (size_t)split * (CC / NSPLIT)) * 512 + (w & 1) * 8192;
  {
    const char* src = w < 4 ? q + ((size_t)grp * 64 + 16 * w) * 512 : kb;
    const uint32_t dst = lds0 + (w < 4 ? P_OFF + w * PW : (w & 1) * 8192);
    dma4(src, dst, ko[0], ko[1], ko[2], ko[3]);
    dma4(src, dst + 4096, ko[4], ko[5], ko[6], ko[7]);
  }
  unsigned* flags = (unsigned*)(ws + WS_FLAG) + (size_t)grp * 32;

  if (w >= 4) {
    const int h = w & 1;
    uint32_t vo[4];
    const char* vb = vc + ((size_t)grp * CC + (size_t)split * (CC / NSPLIT)) * 512 + h * 32768;
    auto issue = [&](int s) {
      const uint32_t dst = lds0 + ((s & 3) + (s >= 28 ? 4 : 0)) * SLOT + h * 8192;
      if (s < 16) {
        const char* src = kb + (size_t)s * SLOT;
        dma4(src, dst, ko[0], ko[1], ko[2], ko[3]);
        dma4(src, dst + 4096, ko[4], ko[5], ko[6], ko[7]);
      } else {
        const char* src = vb + (size_t)(s & 3) * 65536 + ((split + 1 + (s >> 2)) & 3) * 128;
        dma4(src, dst, vo[0], vo[1], vo[2], vo[3]);
        dma4(src + 16384, dst + 4096, vo[0], vo[1], vo[2], vo[3]);
      }
    };
#define LTS(i)
    LTS(96);
    issue(1); issue(2);
    LTS(97);
    {
#pragma unroll
      for (int j = 0; j < 4; ++j) vo[j] = hi * 512 + ((pos ^ ((2 * j + hi) << h)) << 4);
      dma4((h ? vt : kt) + ((size_t)grp * 32 + 8 * split) * 512, lds0 + TK_OFF + h * 4096, vo[0], vo[1], vo[2], vo[3]);
    }
    unsigned beat = 0;
    asm volatile("global_load_dword %0, %1, %2 sc1" : "=v"(beat) : "v"(split * 4), "s"(flags) : "memory");
    LTS(98);
#pragma unroll
    for (int j = 0; j < 4; ++j) vo[j] = (lane >> 3) * 512 + (((lane & 7) ^ (((lane >> 4) & 3) << 1)) << 4) + j * 3072;
    wait_vm<21>();
    LTS(99);
    issue(3);
    LTS(0);
    asm volatile("s_barrier" ::: "memory");
    LTS(1); LTS(2);
#pragma unroll 1
    for (int s = 1; s < 25; ++s) {
      if (s < 3) wait_vm<21>(); else wait_vm<16>();
      LTS(3 * s);
      asm volatile("s_barrier" ::: "memory");
      LTS(3 * s + 1);
      issue(s + 3);
      if (s >= 17 && (s & 1) && h == 1) { asm volatile("" : "+v"(beat)); ++beat; if (lane == 0) st_flag(flags + split, beat); }
      LTS(3 * s + 2);
    }
    wait_vm<16>(); LTS(75); asm volatile("s_barrier" ::: "memory"); LTS(76);
    issue(28); LTS(77);
    wait_vm<16>(); LTS(78); asm volatile("s_barrier" ::: "memory"); LTS(79);
    issue(29); LTS(80);
    wait_vm<16>(); LTS(81); asm volatile("s_barrier" ::: "memory"); LTS(82); issue(30); LTS(83);
    wait_vm<16>(); LTS(84); asm volatile("s_barrier" ::: "memory"); LTS(85); issue(31); LTS(86);
    wait_vm<16>(); LTS(87); asm volatile("s_barrier" ::: "memory"); LTS(88); LTS(89);
    wait_vm<8>();  LTS(90); asm volatile("s_barrier" ::: "memory"); LTS(91); LTS(92);
    wait_vm<0>();  LTS(93); asm volatile("s_barrier" ::: "memory"); LTS(94); LTS(95);
    return;
  }

  const int r = lane & 15, g = lane >> 4;
  const uint32_t pw = P_OFF + w * PW;
  const int wg = grp * NSPLIT + split;
  const __amdgpu_buffer_rsrc_t rs = __builtin_amdgcn_make_buffer_rsrc(ws, 0, (int)WS_END, 0x00020000);
  uint32_t kra = r * 512 + ((g ^ r) << 4);
  uint32_t tka = TK_OFF + (r & 7) * 512 + ((g ^ (r & 7)) << 4);
  uint32_t pa = pw + lane * 16;
  const int vrow = 4 * g + (r >> 2);
  uint32_t vra = vrow * 128 + (((vrow >> 1) & 3) << 5) + ((r & 3) << 3);
  uint32_t tva = TV_OFF + (vrow & 7) * 512 + ((vrow & 7) << 5) + ((r & 3) << 3);
  uint32_t pw0 = (uint32_t)WS_PO + (uint32_t)((wg * 4 + w) * 4) * 2048u + lane * 16;
  uint32_t mla = (uint32_t)WS_ML + (uint32_t)(grp * 256 + 16 * w + r) * 8u;
  uint32_t pfa = (uint32_t)WS_PO + (uint32_t)((grp * 16 + w) * 4 + split) * 2048u + lane * 16;
  uint32_t ooff = (uint32_t)(((grp * 64 + 16 * w + r) * DD + 64 * split + 4 * g) * 2);
  uint32_t fo = (lane & 7) * 4;
  PIN(kra); PIN(tka); PIN(pa); PIN(vra); PIN(tva); PIN(pw0); PIN(mla); PIN(pfa); PIN(ooff); PIN(fo);
  asm volatile("s_waitcnt vmcnt(0)" ::"s"(out), "s"(mask) : "memory");
  bf16x8 qf[8];
#pragma unroll
  for (int s = 0; s < 8; ++s) qf[s] = *(const LAS bf16x8*)((LAS char*)smem + ((pw + kra) ^ (64 * (s & 3))) + 256 * (s >> 2));
  const unsigned f0 = ld_flag(flags + (lane & 7));
  const u32x2 mk = *(const GAS u32x2*)(mask + (size_t)(grp >> 3) * 2048 + (16 * (w & 1) + r) * 64 + 16 * split + 8 * (g & 1));
  TS(1);
  bar();

  const __bf16 one = (__bf16)1.0f;
  bf16x8 ones = {one, one, one, one, one, one, one, one};
  PIN(ones);
  const float ninf = -__builtin_inff();
  f32x4 s0 = {ninf, ninf, ninf, ninf}, s1 = s0, ls = {0.f, 0.f, 0.f, 0.f};
  float m = -3.0e38f;
#pragma unroll 1
  for (int t = 0; t < 17; ++t) {
    SBAR();
    const uint32_t kq = t < 16 ? (t & 3) * SLOT + kra : tka;
    bf16x8 a[16];
    auto rd = [&](int i) { a[i] = *(const LAS bf16x8*)((LAS char*)smem + ((kq ^ (64 * ((i >> 1) & 3))) + 256 * (i >> 3) + 8192 * (i & 1))); };
#pragma unroll
    for (int i = 0; i < 6; ++i) rd(i);
    SBAR();
    const float x[8] = {s0[0], s0[1], s0[2], s0[3], s1[0], s1[1], s1[2], s1[3]};
    float e[8];
#pragma unroll
    for (int i = 0; i < 4; ++i) e[i] = __builtin_amdgcn_exp2f(__builtin_fmaf(x[i], SC, -m));
    e[4] = __builtin_fmaf(x[4], SC, -m);
    u32x4 pf;
    f32x4 n[2] = {{0.f, 0.f, 0.f, 0.f}, {0.f, 0.f, 0.f, 0.f}};
    SBAR();
#pragma unroll
    for (int i = 0; i < 16; ++i) {
      n[i & 1] = __builtin_amdgcn_mfma_f32_16x16x32_bf16(a[i], qf[i >> 1], n[i & 1], 0, 0, 0);
      SBAR();
      if (i + 6 < 16) rd(i + 6);
      if (i < 4) e[4 + i] = __builtin_amdgcn_exp2f(e[4 + i]);
      if (i < 3) e[5 + i] = __builtin_fmaf(x[5 + i], SC, -m);
      if (i == 4) { pf[0] = pk2(e[0], e[1]); pf[1] = pk2(e[2], e[3]); }
      if (i == 5) { pf[2] = pk2(e[4], e[5]); pf[3] = pk2(e[6], e[7]); }
      if (i == 10) *(LAS u32x4*)((LAS char*)smem + pa + t * 1024) = pf;
      SBAR();
    }
    ls = __builtin_amdgcn_mfma_f32_16x16x32_bf16(ones, __builtin_bit_cast(bf16x8, pf), ls, 0, 0, 0);
    SBAR();
    s0 = n[0]; s1 = n[1];
    if (t == 16) {
      u32x2 mq = mk;
      PIN(mq);
      const float mv[4] = {__uint_as_float(mq[0] << 16), __uint_as_float(mq[0] & 0xffff0000u), __uint_as_float(mq[1] << 16), __uint_as_float(mq[1] & 0xffff0000u)};
#pragma unroll
      for (int j = 0; j < 4; ++j) { s0[j] = g < 2 ? s0[j] + (1.0f - mv[j]) * MASKR : ninf; s1[j] = ninf; }
    }
    const float mx = vmax(vmax(vmax(vmax(s0[0], s0[1]), s0[2]), vmax(vmax(s0[3], s1[0]), s1[1])), vmax(s1[2], s1[3])) * SC;
    if (__any(!(mx <= m + SLACK))) {
      const float mn = vmax(m, __builtin_ceilf(xg_max(mx)));
      const float df = fminf(mn - m, 255.f);
      m = mn;
      ls *= __builtin_amdgcn_exp2f(-df);
      const unsigned short d = (unsigned short)((unsigned)df << 7);
      const u16x8 dv = {d, d, d, d, d, d, d, d};
#pragma unroll 1
      for (int i = 1; i <= t; ++i) {
        LAS u16x8* pp = (LAS u16x8*)((LAS char*)smem + pa + i * 1024);
        *pp = __builtin_elementwise_sub_sat(*pp, dv);
      }
    }
    if (t < 16) bar();
  }
  TS(2);
  bf16x8 P[16];
#pragma unroll
  for (int i = 0; i < 16; ++i) P[i] = *(const LAS bf16x8*)((LAS char*)smem + pa + (i + 1) * 1024);
  SBAR();
  s16x4 pt;
  {
    const u32x2 pp = {pk2(__builtin_amdgcn_exp2f(__builtin_fmaf(s0[0], SC, -m)), __builtin_amdgcn_exp2f(__builtin_fmaf(s0[1], SC, -m))),
                      pk2(__builtin_amdgcn_exp2f(__builtin_fmaf(s0[2], SC, -m)), __builtin_amdgcn_exp2f(__builtin_fmaf(s0[3], SC, -m)))};
    pt = __builtin_bit_cast(s16x4, pp);
    ls = __builtin_amdgcn_mfma_f32_16x16x16bf16_1k(__builtin_bit_cast(s16x4, __builtin_shufflevector(ones, ones, 0, 1, 2, 3)), pt, ls, 0, 0, 0);
  }
  const float l = ls[0];
  wait_vm<0>();
  if (g == 0) __builtin_amdgcn_raw_buffer_store_b64(u32x2{__float_as_uint(m), __float_as_uint(l)}, rs, mla, split * 512, 16);
  poll_issue(out, ooff, lds0 + PL_OFF + w * 256);

  const bool other = (lane & 3) != split;
  f32x4 o[4];
  u32x4 po[2], a[3][2]; u32x2 st[3];
  unsigned beat = __builtin_amdgcn_readlane(f0, split) + 4096u;
  bool got = false, cok = false;
  auto poll = [&]() { poll_issue(flags, fo, lds0 + PL_OFF + w * 256); };
  auto changed = [&](int half, unsigned v) { return __all(!other || (lane & 4) != half || v != f0) != 0; };
  auto seen = [&](int half) {
    wait_vm<0>();
    return changed(half, *(const LAS unsigned*)((LAS char*)smem + PL_OFF + w * 256 + lane * 4));
  };
  auto fetch = [&]() {
#pragma unroll
    for (int k = 0; k < 3; ++k) {
      const int so = (split + 1 + k) & 3;
      st[k] = __builtin_amdgcn_raw_buffer_load_b64(rs, mla, so * 512, 16);
      a[k][0] = __builtin_amdgcn_raw_buffer_load_b128(rs, pfa, so * 32768, 16);
      a[k][1] = __builtin_amdgcn_raw_buffer_load_b128(rs, pfa + 1024, so * 32768, 16);
    }
  };
#pragma unroll 1
  for (int b = 0; b < 4; ++b) {
    const int blk = (split + 1 + b) & 3;
    const uint32_t vb = vra + (b == 3 ? P_OFF : 0);
#pragma unroll
    for (int db = 0; db < 4; ++db) {
      const bf16x4 tv = __builtin_amdgcn_ds_read_tr16_b64_v4bf16((LAS bf16x4*)((LAS char*)smem + (tva ^ (32 * (4 * blk + db)))));
      o[db] = __builtin_amdgcn_mfma_f32_16x16x16bf16_1k(__builtin_bit_cast(s16x4, tv), pt, f32x4{0.f, 0.f, 0.f, 0.f}, 0, 0, 0);
    }
#pragma unroll
    for (int qq = 0; qq < 4; ++qq) {
      if (qq > 0 || b > 0) bar();
      if (b == 3) {
        if (qq == 1 && w == 0) {
          for (unsigned it = 1; !cok; ++it) {
            if ((it & 15) == 0) { ++beat; if (lane == 0) st_flag(flags + split, beat); }
            __builtin_amdgcn_s_sleep(1);
            cok = changed(0, ld_flag(flags + (lane & 7)));
          }
          if (lane == 0) st_flag(flags + 4 + split, __builtin_amdgcn_readlane(f0, 4 + split) + 1u);
          TS(3);
        }
        if (qq == 2 || (qq == 3 && !got)) poll();
      }
      SBAR();
      bf16x4 lo[16], hi4[16];
      auto rv = [&](int j) {
        const uint32_t ad = (vb ^ (32 * (j & 3))) + qq * SLOT + (j >> 2) * 4096;
        lo[j] = __builtin_amdgcn_ds_read_tr16_b64_v4bf16((LAS bf16x4*)((LAS char*)smem + ad));
        hi4[j] = __builtin_amdgcn_ds_read_tr16_b64_v4bf16((LAS bf16x4*)((LAS char*)smem + ad + 2048));
      };
#pragma unroll
      for (int j = 0; j < 6; ++j) rv(j);
      SBAR();
#pragma unroll
      for (int j = 0; j < 16; ++j) {
        o[j & 3] = __builtin_amdgcn_mfma_f32_16x16x32_bf16(__builtin_shufflevector(lo[j], hi4[j], 0, 1, 2, 3, 4, 5, 6, 7), P[4 * qq + (j >> 2)], o[j & 3], 0, 0, 0);
        SBAR();
        if (j + 6 < 16) rv(j + 6);
        SBAR();
      }
      if ((b == 1 || b == 2) && (qq & 1) == 0)
        PST(po[qq >> 1], pw0 + (qq >> 1) * 1024, ((blk + 3) & 3) * 2048);
      if (b == 3) {
        if (qq == 0) cok = seen(0);
        if (qq == 2 && seen(4)) { got = true; fetch(); }
      }
    }
    po[0] = u32x4{pk2(o[0][0], o[0][1]), pk2(o[0][2], o[0][3]), pk2(o[1][0], o[1][1]), pk2(o[1][2], o[1][3])};
    po[1] = u32x4{pk2(o[2][0], o[2][1]), pk2(o[2][2], o[2][3]), pk2(o[3][0], o[3][1]), pk2(o[3][2], o[3][3])};
    if (b == 2) {
      PST(po[0], pw0, blk * 2048);
      PST(po[1], pw0 + 1024, blk * 2048);
      poll();
    }
  }
  TS(4);
  if (!got) {
    got = seen(4);
    for (unsigned it = 1; !got; ++it) {
      if (w == 0 && (it & 15) == 0) { ++beat; if (lane == 0) st_flag(flags + split, beat); }
      got = changed(4, ld_flag(flags + (lane & 7)));
    }
    fetch();
  }
  TS(5);
  {
    float M = m;
#pragma unroll
    for (int k = 0; k < 3; ++k) M = vmax(M, __uint_as_float(st[k][0]));
    float w0 = __builtin_amdgcn_exp2f(m - M);
    float L = w0 * l, wt[3];
#pragma unroll
    for (int k = 0; k < 3; ++k) { wt[k] = __builtin_amdgcn_exp2f(__uint_as_float(st[k][0]) - M); L += wt[k] * __uint_as_float(st[k][1]); }
    const float inv = 1.0f / L;
    w0 *= inv; wt[0] *= inv; wt[1] *= inv; wt[2] *= inv;
    char* orow = (char*)out + ooff;
#pragma unroll
    for (int i = 0; i < 4; ++i) {
      f32x4 v = o[i] * w0;
#pragma unroll
      for (int k = 0; k < 3; ++k) {
        const unsigned x0 = a[k][i >> 1][2 * (i & 1)], x1 = a[k][i >> 1][2 * (i & 1) + 1];
        v += f32x4{__uint_as_float(x0 << 16), __uint_as_float(x0 & 0xffff0000u), __uint_as_float(x1 << 16), __uint_as_float(x1 & 0xffff0000u)} * wt[k];
      }
      *(u32x2*)(orow + 32 * i) = u32x2{pk2(v[0], v[1]), pk2(v[2], v[3])};
    }
  }
}
}

extern "C" void kernel_launch(void* const* d_in, const int* in_sizes, int n_in, void* d_out, int out_size, void* d_ws,
                              size_t ws_size, hipStream_t stream) {
  (void)hipFuncSetAttribute((const void*)attn_tree, hipFuncAttributeMaxDynamicSharedMemorySize, LDS_BYTES + 512);
  attn_tree<<<NGROUP * NSPLIT, 384, LDS_BYTES + 512, stream>>>((const char*)d_in[0], (const char*)d_in[1], (const char*)d_in[2],
                                                 (const char*)d_in[3], (const char*)d_in[4], (const char*)d_in[5],
                                                 (char*)d_ws, (unsigned short*)d_out);
}
```

```cpp
#define SAUX 18
#include <hip/hip_runtime.h>
#include <hip/hip_bf16.h>
#include <stdint.h>

namespace {
constexpr int NB = 8, HKV = 8, CC = 2048, DD = 256;
constexpr int NSPLIT = 4;
constexpr int NGROUP = NB * HKV;
constexpr int SLOT = 16384;
constexpr int NRING = 5;
#define LAUX 16
#define OAUX 0
#define CHK_T 10
#define MBM 0
#define DEPTH 2
constexpr int TK_OFF = NRING * SLOT;
constexpr int TV_OFF = TK_OFF + 4096;
constexpr int PL_OFF = TV_OFF + 4096;
constexpr int SN_OFF = PL_OFF + 1024;
constexpr int P_OFF = SN_OFF + 1024;
constexpr int PW = 17 * 1024;
constexpr int SY_OFF = P_OFF + 4 * PW;
constexpr int LDS_BYTES = SY_OFF + 64;
constexpr float LOG2E = 1.4426950408889634f;
constexpr float SC = 0.0625f * LOG2E;
constexpr float MASKR = -1.0e9f * 16.0f;
constexpr float SLACK = 10.0f;
constexpr size_t WS_FLAG = 0;
constexpr size_t WS_MB = 65536;
constexpr size_t WS_END = WS_MB + (size_t)256 * 32768;

typedef __attribute__((ext_vector_type(8))) __bf16 bf16x8;
typedef __attribute__((ext_vector_type(4))) __bf16 bf16x4;
typedef __attribute__((ext_vector_type(4))) short s16x4;
typedef __attribute__((ext_vector_type(8))) unsigned short u16x8;
typedef __attribute__((ext_vector_type(4))) float f32x4;
typedef __attribute__((ext_vector_type(4))) unsigned u32x4;
typedef __attribute__((ext_vector_type(2))) unsigned u32x2;
#define LAS __attribute__((address_space(3)))
#define GAS __attribute__((address_space(1)))
#define DI __device__ __forceinline__
#define SBAR() __builtin_amdgcn_sched_barrier(0)
#define PIN(x) asm volatile("" : "+v"(x))
#define CFENCE() asm volatile("" ::: "memory")
#define WD
#define TS(i)

DI void dma4(const char* sbase, uint32_t m0v, uint32_t v0, uint32_t v1, uint32_t v2, uint32_t v3) {
  uint32_t keep;
  asm volatile(
      "s_mov_b32 %0, m0\n\ts_mov_b32 m0, %6\n\ts_nop 0\n\t"
      "global_load_lds_dwordx4 %1, %5 nt\n\t"
      "global_load_lds_dwordx4 %2, %5 offset:1024 nt\n\t"
      "global_load_lds_dwordx4 %3, %5 offset:2048 nt\n\t"
      "global_load_lds_dwordx4 %4, %5 offset:3072 nt\n\t"
      "s_mov_b32 m0, %0"
      : "=&s"(keep) : "v"(v0), "v"(v1), "v"(v2), "v"(v3), "s"(sbase), "s"(m0v) : "memory");
}
DI void dma1(const char* sbase, uint32_t m0v, uint32_t v0) {
  uint32_t keep;
  asm volatile("s_mov_b32 %0, m0\n\ts_mov_b32 m0, %3\n\ts_nop 0\n\t"
               "global_load_lds_dwordx4 %1, %2 nt\n\t"
               "s_mov_b32 m0, %0"
               : "=&s"(keep) : "v"(v0), "s"(sbase), "s"(m0v) : "memory");
}
DI void poll_issue(const void* base, uint32_t off, uint32_t m0v) {
  uint32_t keep;
  asm volatile("s_mov_b32 %0, m0\n\ts_mov_b32 m0, %3\n\ts_nop 0\n\t"
               "global_load_lds_dword %1, %2 sc1\n\t"
               "s_mov_b32 m0, %0"
               : "=&s"(keep) : "v"(off), "s"(base), "s"(m0v) : "memory");
}
template <int N> DI void wait_vm() { asm volatile("s_waitcnt vmcnt(%0)" ::"i"(N) : "memory"); }
DI void bar() { asm volatile("s_waitcnt lgkmcnt(0)\n\ts_barrier" ::: "memory"); }
DI float vmax(float a, float b) { return __builtin_elementwise_maximum(a, b); }
DI float xg_max(float x) {
  auto s = __builtin_amdgcn_permlane16_swap(__float_as_uint(x), __float_as_uint(x), false, false);
  x = vmax(__uint_as_float(s[0]), __uint_as_float(s[1]));
  auto t = __builtin_amdgcn_permlane32_swap(__float_as_uint(x), __float_as_uint(x), false, false);
  return vmax(__uint_as_float(t[0]), __uint_as_float(t[1]));
}
DI unsigned ld_flag(const unsigned* p) { return __hip_atomic_load((const GAS unsigned*)p, __ATOMIC_RELAXED, __HIP_MEMORY_SCOPE_AGENT); }
DI void st_flag(unsigned* p, unsigned v) { __hip_atomic_store((GAS unsigned*)p, v, __ATOMIC_RELAXED, __HIP_MEMORY_SCOPE_AGENT); }
DI unsigned long long ld_lds64(LAS char* p) { return __hip_atomic_load((LAS unsigned long long*)p, __ATOMIC_RELAXED, __HIP_MEMORY_SCOPE_WORKGROUP); }
DI void st_lds(LAS char* p, unsigned v) { __hip_atomic_store((LAS unsigned*)p, v, __ATOMIC_RELAXED, __HIP_MEMORY_SCOPE_WORKGROUP); }
DI unsigned umin(unsigned a, unsigned b) { return a < b ? a : b; }
DI int lo_min(unsigned long long x) { return __builtin_amdgcn_readfirstlane((int)umin((unsigned)x, (unsigned)(x >> 32))); }
DI unsigned pk2(float a, float b) { typedef __attribute__((ext_vector_type(2))) __bf16 bf16x2; const bf16x2 v = {(__bf16)a, (__bf16)b}; return __builtin_bit_cast(unsigned, v); }
DI f32x4 unpk(unsigned x0, unsigned x1) { return f32x4{__uint_as_float(x0 << 16), __uint_as_float(x0 & 0xffff0000u), __uint_as_float(x1 << 16), __uint_as_float(x1 & 0xffff0000u)}; }
DI uint32_t ring_next(uint32_t x) { return x == (NRING - 1) * SLOT ? 0 : x + SLOT; }

__global__ void __launch_bounds__(384)
attn_tree(const char* __restrict__ q, const char* __restrict__ kc, const char* __restrict__ vc,
          const char* __restrict__ kt, const char* __restrict__ vt, const char* __restrict__ mask,
          char* __restrict__ ws, unsigned short* __restrict__ out) {
  extern __shared__ __attribute__((aligned(1024))) char smem_[];
  LAS char* const smem = (LAS char*)smem_;
  const int tid = threadIdx.x, lane = tid & 63;
  const int w = __builtin_amdgcn_readfirstlane(tid >> 6) - 2;
  const int bid = blockIdx.x;
  TS(0);
  const int grp = (bid & 7) * 8 + (bid >> 5), split = (bid >> 3) & 3;
  const uint32_t lds0 = (uint32_t)(uintptr_t)smem;
  const int hi = lane >> 5, pos = lane & 31;
  const size_t kvoff = ((size_t)grp * CC + (size_t)split * (CC / NSPLIT)) * 512;
  uint32_t ko[8];
#pragma unroll
  for (int j = 0; j < 8; ++j) { ko[j] = (j >> 2) * 4096 + hi * 512 + ((pos ^ (2 * j + hi)) << 4); PIN(ko[j]); }
  st_lds(smem + SY_OFF + (w & 7) * 4, 0u);
  st_lds(smem + SY_OFF + 16 + (w & 1) * 4, 0u);
  asm volatile("" ::"s"(q), "s"(kc), "s"(vc), "s"(kt), "s"(vt), "s"(mask), "s"(ws), "s"(out));
  const char* kb = kc + kvoff + (w & 1) * 8192;
  {
    const char* src = w >= 0 ? q + ((size_t)grp * 64 + 16 * w) * 512 : kb;
    const uint32_t dst = lds0 + (w >= 0 ? P_OFF + w * PW : (w & 1) * 8192);
    dma4(src, dst, ko[0], ko[1], ko[2], ko[3]);
    dma4(src, dst + 4096, ko[4], ko[5], ko[6], ko[7]);
  }
  if (w >= 0) {
  bar();
  const int r = lane & 15, g = lane >> 4;
  unsigned* flags = (unsigned*)(ws + WS_FLAG) + (size_t)grp * 32;
  const __amdgpu_buffer_rsrc_t rs = __builtin_amdgcn_make_buffer_rsrc(ws, 0, (int)WS_END, 0x00020000);
  uint32_t kra = r * 512 + ((g ^ r) << 4);
  uint32_t pa = P_OFF + w * PW + lane * 16;
  const int vrow = 4 * g + (r >> 2);
  uint32_t vra = vrow * 128 + (((vrow >> 1) & 3) << 5) + ((r & 3) << 3);
  uint32_t tva = TV_OFF + (vrow & 7) * 512 + ((vrow & 7) << 5) + ((r & 3) << 3);
  const uint32_t mbg = (uint32_t)WS_MB + (uint32_t)(grp * 16 + w) * 8192u;
  uint32_t mbl = mbg + lane * 16;
  uint32_t mla = mbg + 6144 + r * 8;
  uint32_t ooff = (uint32_t)(((grp * 64 + 16 * w + r) * DD + 64 * split + 4 * g) * 2);
  uint32_t fo = (lane & 7) * 4;
  uint32_t tka = TK_OFF + (r & 7) * 512 + ((g ^ (r & 7)) << 4);
  uint32_t sya = SY_OFF;
  PIN(kra); PIN(pa); PIN(vra); PIN(tva); PIN(mbl); PIN(mla); PIN(ooff); PIN(fo); PIN(tka); PIN(sya);
  auto landed = [&]() { return ld_lds64(smem + sya + 24); };
  auto await = [&](int s) { while (lo_min(landed()) <= s) { __builtin_amdgcn_s_sleep(1); WD; } CFENCE(); };
  wait_vm<0>();
  bf16x8 qf[8];
#pragma unroll
  for (int s = 0; s < 8; ++s) qf[s] = *(const LAS bf16x8*)(smem + ((P_OFF + w * PW + kra) ^ (64 * (s & 3))) + 256 * (s >> 2));
  if (w == 0) poll_issue(flags, fo, lds0 + SN_OFF);
  dma1(mask + (size_t)(grp >> 3) * 2048 + (w & 1) * 1024, lds0 + P_OFF + w * PW + 15 * 1024, lane * 16);
  TS(1);

  const float ninf = -__builtin_inff();
  f32x4 s0 = {ninf, ninf, ninf, ninf}, s1 = s0, ls = {0.f, 0.f, 0.f, 0.f};
  float m = -3.0e38f;
  const __bf16 one = (__bf16)1.0f;
  bf16x8 ones = {one, one, one, one, one, one, one, one};
  PIN(ones);
  unsigned beat = 0;
  uint32_t ring = 0;
  int slot = 0;
  auto settle = [&](int nrec) {
    const float mx = vmax(vmax(vmax(vmax(s0[0], s0[1]), s0[2]), vmax(vmax(s0[3], s1[0]), s1[1])), vmax(s1[2], s1[3])) * SC;
    if (__any(!(mx <= m + SLACK))) {
      const float mn = vmax(m, __builtin_ceilf(xg_max(mx)));
      const float df = fminf(mn - m, 255.f);
      m = mn;
      ls *= __builtin_amdgcn_exp2f(-df);
      const unsigned short d = (unsigned short)((unsigned)df << 7);
      const u16x8 dv = {d, d, d, d, d, d, d, d};
#pragma unroll 1
      for (int i = 0; i < nrec; ++i) {
        LAS u16x8* pp = (LAS u16x8*)(smem + pa + i * 1024);
        *pp = __builtin_elementwise_sub_sat(*pp, dv);
      }
    }
  };
  {
    bf16x8 a[16];
    uint32_t kq = kra;
    auto rd = [&](int i) { a[i] = *(const LAS bf16x8*)(smem + ((kq ^ (64 * ((i >> 1) & 3))) + 256 * (i >> 3) + 8192 * (i & 1))); };
    await(0);
#pragma unroll
    for (int i = 0; i < 6; ++i) rd(i);
    ring = SLOT; slot = 1;
#pragma unroll 1
    for (int t = 0; t < 17; ++t) {
      float e[8];
      u32x4 pf;
      unsigned long long fv;
      bool ready;
      f32x4 n[2] = {{0.f, 0.f, 0.f, 0.f}, {0.f, 0.f, 0.f, 0.f}};
      SBAR();
#pragma unroll
      for (int i = 0; i < 16; ++i) {
        n[i & 1] = __builtin_amdgcn_mfma_f32_16x16x32_bf16(a[i], qf[i >> 1], n[i & 1], 0, 0, 0);
        SBAR();
        if (i == 4) fv = landed();
        if (i == 10) {
          ready = (int)(t == 7) | (int)(lo_min(fv) > slot);
          kq = t == 7 ? tka : ring + kra;
        }
        rd((i + 6) & 15);
        if (i == 9) st_lds(smem + sya + 4 * w, (unsigned)slot);
        if (i == 1) {
          if (t == 9) {
            const u32x2 mk = *(const LAS u32x2*)(smem + P_OFF + w * PW + 15 * 1024 + r * 64 + 16 * split + 8 * (g & 1));
            const f32x4 mv = unpk(mk[0], mk[1]);
#pragma unroll
            for (int j = 0; j < 4; ++j) { s0[j] = g < 2 ? s0[j] + (1.0f - mv[j]) * MASKR : ninf; s1[j] = ninf; }
          }
          settle(t - 1);
        }
        if (i >= 2 && i < 10) { e[i - 2] = __builtin_amdgcn_exp2f(__builtin_fmaf(i < 6 ? s0[(i - 2) & 3] : s1[(i - 2) & 3], SC, -m)); PIN(e[i - 2]); }
        if (i == 10) { pf[0] = pk2(e[0], e[1]); pf[1] = pk2(e[2], e[3]); }
        if (i == 11) { pf[2] = pk2(e[4], e[5]); pf[3] = pk2(e[6], e[7]); }
        if (i == 12) *(LAS u32x4*)(smem + pa + (t == 0 ? 16 : t - 1) * 1024) = pf;
        SBAR();
      }
      ls = __builtin_amdgcn_mfma_f32_16x16x32_bf16(ones, __builtin_bit_cast(bf16x8, pf), ls, 0, 0, 0);
      SBAR();
      s0 = n[0]; s1 = n[1];
      if (t == 6) wait_vm<0>();
      SBAR();
      if (t == CHK_T && w == 0 && lane == split) st_flag(flags + split, *(const LAS unsigned*)(smem + SN_OFF + lane * 4) + 1u);
      if (!ready) {
        await(slot);
#pragma unroll
        for (int i = 0; i < 6; ++i) rd(i);
      }
      if (t != 7) { ring = ring_next(ring); ++slot; }
    }
  }
  TS(2);
  f32x4 o[4];
  bf16x4 lo[16], hi4[16];
  bf16x8 P[4];
  uint32_t vb = vra + (16 % NRING) * SLOT, pq = pa;
  auto rv = [&](int j) {
    const uint32_t ad = (vb ^ (32 * (j & 3))) + (j >> 2) * 4096;
    lo[j] = __builtin_amdgcn_ds_read_tr16_b64_v4bf16((LAS bf16x4*)(smem + ad));
    hi4[j] = __builtin_amdgcn_ds_read_tr16_b64_v4bf16((LAS bf16x4*)(smem + ad + 2048));
  };
  auto rp = [&](int i) { P[i] = *(const LAS bf16x8*)(smem + pq + i * 1024); };
  bf16x4 tv[4];
  auto rtv = [&](int blk) {
#pragma unroll
    for (int db = 0; db < 4; ++db) tv[db] = __builtin_amdgcn_ds_read_tr16_b64_v4bf16((LAS bf16x4*)(smem + (tva ^ (32 * (4 * blk + db)))));
  };
  settle(16);
  const s16x4 pt = *(const LAS s16x4*)(smem + pa + 8 * 1024);
  rtv((split + 1) & 3);
  SBAR();
  rp(0); rp(1);
#pragma unroll
  for (int j = 0; j < 6; ++j) { rv(j); SBAR(); }
  const unsigned f0 = *(const LAS unsigned*)(smem + SN_OFF + lane * 4);
  SBAR();
  {
    u32x4 pf;
#pragma unroll
    for (int i = 0; i < 2; ++i) {
      pf[i] = pk2(__builtin_amdgcn_exp2f(__builtin_fmaf(s0[2 * i], SC, -m)), __builtin_amdgcn_exp2f(__builtin_fmaf(s0[2 * i + 1], SC, -m)));
      pf[2 + i] = pk2(__builtin_amdgcn_exp2f(__builtin_fmaf(s1[2 * i], SC, -m)), __builtin_amdgcn_exp2f(__builtin_fmaf(s1[2 * i + 1], SC, -m)));
    }
    *(LAS u32x4*)(smem + pa + 16 * 1024) = pf;
    ls = __builtin_amdgcn_mfma_f32_16x16x32_bf16(ones, __builtin_bit_cast(bf16x8, pf), ls, 0, 0, 0);
  }
  auto otree = [&]() {
#pragma unroll
    for (int db = 0; db < 4; ++db) o[db] = __builtin_amdgcn_mfma_f32_16x16x16bf16_1k(__builtin_bit_cast(s16x4, tv[db]), pt, f32x4{0.f, 0.f, 0.f, 0.f}, 0, 0, 0);
  };
  otree();
  auto changed = [&](int half, unsigned v) { return __all((lane & 3) == split || (lane & 4) != half || v != f0) != 0; };
  const int mine = split * 32768;
  if (g == 0) __builtin_amdgcn_raw_buffer_store_b64(u32x2{__float_as_uint(m), __float_as_uint(ls[0])}, rs, mla, mine, 16);

  u32x4 ea[3][2]; u32x2 st[3];
  bool rok = false;
  beat = 1 + 4096 * w;
  auto heartbeat = [&]() { ++beat; if (lane == split) st_flag(flags + split, f0 + beat); };
#pragma unroll 1
  for (int v = 0; v < 16; ++v) {
    unsigned long long fv;
    bool ready;
    SBAR();
#pragma unroll
    for (int j = 0; j < 16; ++j) {
      o[j & 3] = __builtin_amdgcn_mfma_f32_16x16x32_bf16(__builtin_shufflevector(lo[j], hi4[j], 0, 1, 2, 3, 4, 5, 6, 7), P[j >> 2], o[j & 3], 0, 0, 0);
      SBAR();
      if (j == 1) rp(2);
      if (j == 4) fv = landed();
      if (j == 5) rp(3);
      if (j == 10) {
        ready = (int)(v == 15) | (int)(lo_min(fv) > slot);
        vb = ring + vra;
        pq = pa + ((v + 1) & 3) * 4096 + (((v + 1) & 2) << 9);
        rp(0);
      }
      if (j == 14) rp(1);
      rv((j + 6) & 15);
      if (j == 9) st_lds(smem + sya + 4 * w, (unsigned)slot);
      SBAR();
    }
    if ((v & 3) == 3 && v < 12) {
      const int blk = (split + 1 + (v >> 2)) & 3;
      rtv((blk + 1) & 3);
      const u32x4 p0 = {pk2(o[0][0], o[0][1]), pk2(o[0][2], o[0][3]), pk2(o[1][0], o[1][1]), pk2(o[1][2], o[1][3])};
      const u32x4 p1 = {pk2(o[2][0], o[2][1]), pk2(o[2][2], o[2][3]), pk2(o[3][0], o[3][1]), pk2(o[3][2], o[3][3])};
      const int so = blk * 32768 + (v >> 2) * 2048;
      __builtin_amdgcn_raw_buffer_store_b128(p0, rs, mbl, so, SAUX);
      __builtin_amdgcn_raw_buffer_store_b128(p1, rs, mbl + 1024, so, SAUX);
      otree();
    }
    SBAR();
    if (v == 9 && w == 0) {
      wait_vm<0>();
      if (changed(0, *(const LAS unsigned*)(smem + PL_OFF + lane * 4))) st_lds(smem + sya + 20, 1u);
    }
    SBAR();
    if (v == 12) {
      wait_vm<0>();
      unsigned long long c = 0;
      if (lane == 0) c = __hip_atomic_fetch_add((LAS unsigned long long*)(smem + sya + 16), 1ull, __ATOMIC_RELAXED, __HIP_MEMORY_SCOPE_WORKGROUP);
      const unsigned c0 = __builtin_amdgcn_readfirstlane((unsigned)c);
      bool cok = __builtin_amdgcn_readfirstlane((unsigned)(c >> 32)) != 0;
      if (c0 == 3) {
        for (unsigned it = 1; !cok; ++it) {
          if ((it & 15) == 0) heartbeat();
          __builtin_amdgcn_s_sleep(1);
          cok = changed(0, ld_flag(flags + (lane & 7)));
          WD;
        }
        if (lane == 4 + split) st_flag(flags + 4 + split, f0 + 1u);
      }
      TS(3);
    }
    SBAR();
    if (!ready) {
      await(slot);
#pragma unroll
      for (int j = 0; j < 6; ++j) { rv(j); SBAR(); }
    }
    SBAR();
    if (v == 13 || (v == 5 && w == 0)) poll_issue(flags, fo, lds0 + PL_OFF + w * 256);
    SBAR();
    if (v == 14) {
      wait_vm<0>();
      rok = changed(4, *(const LAS unsigned*)(smem + PL_OFF + w * 256 + lane * 4));
#pragma unroll
      for (int k = 0; k < 3; ++k) st[k] = __builtin_amdgcn_raw_buffer_load_b64(rs, mla, ((split + 3 - k) & 3) * 32768, 16);
#pragma unroll
      for (int k = 0; k < 6; ++k) ea[k >> 1][k & 1] = __builtin_amdgcn_raw_buffer_load_b128(rs, mbl + k * 1024, mine, LAUX);
    }
    ring = ring_next(ring); ++slot;
  }
  TS(4);
  if (!rok) {
    unsigned it = 0;
    do { if ((++it & 15) == 0) heartbeat(); WD; } while (!changed(4, ld_flag(flags + (lane & 7))));
    u32x4 eb[3][2]; u32x2 sb[3];
#pragma unroll
    for (int k = 0; k < 3; ++k) sb[k] = __builtin_amdgcn_raw_buffer_load_b64(rs, mla, ((split + 3 - k) & 3) * 32768, 16);
#pragma unroll
    for (int k = 0; k < 6; ++k) eb[k >> 1][k & 1] = __builtin_amdgcn_raw_buffer_load_b128(rs, mbl + k * 1024, mine, LAUX);
#pragma unroll
    for (int k = 0; k < 3; ++k) st[k] = sb[k];
#pragma unroll
    for (int k = 0; k < 6; ++k) ea[k >> 1][k & 1] = eb[k >> 1][k & 1];
  }
  TS(5);
  {
    float M = m;
#pragma unroll
    for (int k = 0; k < 3; ++k) M = vmax(M, __uint_as_float(st[k][0]));
    float w0 = __builtin_amdgcn_exp2f(m - M);
    float L = w0 * ls[0], wt[3];
#pragma unroll
    for (int k = 0; k < 3; ++k) { wt[k] = __builtin_amdgcn_exp2f(__uint_as_float(st[k][0]) - M); L += wt[k] * __uint_as_float(st[k][1]); }
    const float inv = 1.0f / L;
    w0 *= inv; wt[0] *= inv; wt[1] *= inv; wt[2] *= inv;
    char* orow = (char*)out + ooff;
#pragma unroll
    for (int i = 0; i < 4; ++i) {
      const int c = i >> 1, e = 2 * (i & 1);
      const f32x4 v = o[i] * w0 + unpk(ea[0][c][e], ea[0][c][e + 1]) * wt[0] + unpk(ea[1][c][e], ea[1][c][e + 1]) * wt[1] + unpk(ea[2][c][e], ea[2][c][e + 1]) * wt[2];
      *(u32x2*)(orow + 32 * i) = u32x2{pk2(v[0], v[1]), pk2(v[2], v[3])};
    }
  }
  return;
  }
  {
    const int h = w & 1;
    uint32_t vo[4];
    const char* vb = vc + kvoff + h * 32768;
    uint32_t dst = lds0 + SLOT + h * 8192;
    auto issue = [&](int s) {
      if (s < 16) {
        const char* src = kb + (size_t)s * SLOT;
        dma4(src, dst, ko[0], ko[1], ko[2], ko[3]);
        dma4(src, dst + 4096, ko[4], ko[5], ko[6], ko[7]);
      } else {
        const char* src = vb + (size_t)(s & 3) * 65536 + ((split + 1 + (s >> 2)) & 3) * 128;
        dma4(src, dst, vo[0], vo[1], vo[2], vo[3]);
        dma4(src + 16384, dst + 4096, vo[0], vo[1], vo[2], vo[3]);
      }
      dst = dst + SLOT >= lds0 + NRING * SLOT ? dst - (NRING - 1) * SLOT : dst + SLOT;
    };
#define LTS(i)
    LTS(96);
    issue(1);
    if (DEPTH >= 3) issue(2);
    if (DEPTH >= 4) issue(3);
    LTS(97);
    {
#pragma unroll
      for (int j = 0; j < 4; ++j) vo[j] = hi * 512 + ((pos ^ ((2 * j + hi) << h)) << 4);
      dma4((h ? vt : kt) + ((size_t)grp * 32 + 8 * split) * 512, lds0 + TK_OFF + h * 4096, vo[0], vo[1], vo[2], vo[3]);
    }
    LTS(98);
    bar();
    LTS(99);
#pragma unroll
    for (int j = 0; j < 4; ++j) vo[j] = (lane >> 3) * 512 + (((lane & 7) ^ (((lane >> 4) & 3) << 1)) << 4) + j * 3072;
    LAS char* full = smem + SY_OFF + 24 + 4 * h;
    auto min4 = [&](unsigned long long x, unsigned long long y) { return __builtin_amdgcn_readfirstlane((int)umin(umin((unsigned)x, (unsigned)(x >> 32)), umin((unsigned)y, (unsigned)(y >> 32)))); };
#pragma unroll 1
    for (int s = 0; s < 32 - DEPTH; ++s) {
      const unsigned long long x = ld_lds64(smem + SY_OFF), y = ld_lds64(smem + SY_OFF + 8);
      SBAR();
      if (s < DEPTH) wait_vm<8 * DEPTH - 4>(); else wait_vm<8 * DEPTH - 8>();
      st_lds(full, (unsigned)(s + 1));
      LTS(3 * s);
      int freed = min4(x, y);
      while (freed <= s + DEPTH - NRING) {
        freed = min4(ld_lds64(smem + SY_OFF), ld_lds64(smem + SY_OFF + 8));
        WD;
      }
      LTS(3 * s + 1);
      issue(s + DEPTH);
      LTS(3 * s + 2);
    }
    if (DEPTH >= 4) { wait_vm<24>(); st_lds(full, 29u); }
    if (DEPTH >= 3) { wait_vm<16>(); st_lds(full, 30u); LTS(87); LTS(88); LTS(89); }
    wait_vm<8>();  st_lds(full, 31u); LTS(90); LTS(91); LTS(92);
    wait_vm<0>();  st_lds(full, 32u); LTS(93); LTS(94); LTS(95);
  }
}
}

extern "C" void kernel_launch(void* const* d_in, const int* in_sizes, int n_in, void* d_out, int out_size, void* d_ws,
                              size_t ws_size, hipStream_t stream) {
  (void)hipFuncSetAttribute((const void*)attn_tree, hipFuncAttributeMaxDynamicSharedMemorySize, LDS_BYTES + 512);
  attn_tree<<<NGROUP * NSPLIT, 384, LDS_BYTES + 512, stream>>>((const char*)d_in[0], (const char*)d_in[1], (const char*)d_in[2],
                                                 (const char*)d_in[3], (const char*)d_in[4], (const char*)d_in[5],
                                                 (char*)d_ws + (MBM == 1 ? ws_size - (16u << 20) : MBM == 5 ? ws_size / 2 : 0), (unsigned short*)d_out);
}
```

```cpp
#include <hip/hip_runtime.h>
#include <hip/hip_bf16.h>
#include <stdint.h>

namespace {
constexpr int NB = 8, HKV = 8, CC = 2048, DD = 256;
constexpr int NSPLIT = 4;
constexpr int NGROUP = NB * HKV;
constexpr int SLOT = 16384;
constexpr int NRING = 5;
constexpr int TK_OFF = NRING * SLOT;
constexpr int TV_OFF = TK_OFF + 4096;
constexpr int PL_OFF = TV_OFF + 4096;
constexpr int SN_OFF = PL_OFF + 1024;
constexpr int P_OFF = SN_OFF + 1024;
constexpr int PW = 17 * 1024;
constexpr int SY_OFF = P_OFF + 4 * PW;
constexpr int LDS_BYTES = SY_OFF + 64;
constexpr float LOG2E = 1.4426950408889634f;
constexpr float SC = 0.0625f * LOG2E;
constexpr float MASKR = -1.0e9f * 16.0f;
constexpr float SLACK = 10.0f;
constexpr size_t WS_FLAG = 0;
constexpr size_t WS_MB = 65536;
constexpr size_t WS_END = WS_MB + (size_t)256 * 32768;
constexpr int MB_G = 32768, MB_W = 2048, MB_S = 8192, MB_K = 2 << 20, MB_I = 1024, ML_B = 6 << 20, ML_G = 2048, ML_W = 128, ML_S = 512;
#define MB_LANE (lane * 16)

typedef __attribute__((ext_vector_type(8))) __bf16 bf16x8;
typedef __attribute__((ext_vector_type(4))) __bf16 bf16x4;
typedef __attribute__((ext_vector_type(4))) short s16x4;
typedef __attribute__((ext_vector_type(8))) unsigned short u16x8;
typedef __attribute__((ext_vector_type(4))) float f32x4;
typedef __attribute__((ext_vector_type(4))) unsigned u32x4;
typedef __attribute__((ext_vector_type(2))) unsigned u32x2;
#define LAS __attribute__((address_space(3)))
#define GAS __attribute__((address_space(1)))
#define DI __device__ __forceinline__
#define SBAR() __builtin_amdgcn_sched_barrier(0)
#define PIN(x) asm volatile("" : "+v"(x))
#define CFENCE() asm volatile("" ::: "memory")

DI void dma4(const char* sbase, uint32_t m0v, uint32_t v0, uint32_t v1, uint32_t v2, uint32_t v3) {
  uint32_t keep;
  asm volatile(
      "s_mov_b32 %0, m0\n\ts_mov_b32 m0, %6\n\ts_nop 0\n\t"
      "global_load_lds_dwordx4 %1, %5 nt\n\t"
      "global_load_lds_dwordx4 %2, %5 offset:1024 nt\n\t"
      "global_load_lds_dwordx4 %3, %5 offset:2048 nt\n\t"
      "global_load_lds_dwordx4 %4, %5 offset:3072 nt\n\t"
      "s_mov_b32 m0, %0"
      : "=&s"(keep) : "v"(v0), "v"(v1), "v"(v2), "v"(v3), "s"(sbase), "s"(m0v) : "memory");
}
DI void dma1(const char* sbase, uint32_t m0v, uint32_t v0) {
  uint32_t keep;
  asm volatile("s_mov_b32 %0, m0\n\ts_mov_b32 m0, %3\n\ts_nop 0\n\t"
               "global_load_lds_dwordx4 %1, %2 nt\n\t"
               "s_mov_b32 m0, %0"
               : "=&s"(keep) : "v"(v0), "s"(sbase), "s"(m0v) : "memory");
}
DI void poll_issue(const void* base, uint32_t off, uint32_t m0v) {
  uint32_t keep;
  asm volatile("s_mov_b32 %0, m0\n\ts_mov_b32 m0, %3\n\ts_nop 0\n\t"
               "global_load_lds_dword %1, %2 sc1\n\t"
               "s_mov_b32 m0, %0"
               : "=&s"(keep) : "v"(off), "s"(base), "s"(m0v) : "memory");
}
template <int N> DI void wait_vm() { asm volatile("s_waitcnt vmcnt(%0)" ::"i"(N) : "memory"); }
DI void bar() { asm volatile("s_waitcnt lgkmcnt(0)\n\ts_barrier" ::: "memory"); }
DI float vmax(float a, float b) { return __builtin_elementwise_maximum(a, b); }
DI float xg_max(float x) {
  auto s = __builtin_amdgcn_permlane16_swap(__float_as_uint(x), __float_as_uint(x), false, false);
  x = vmax(__uint_as_float(s[0]), __uint_as_float(s[1]));
  auto t = __builtin_amdgcn_permlane32_swap(__float_as_uint(x), __float_as_uint(x), false, false);
  return vmax(__uint_as_float(t[0]), __uint_as_float(t[1]));
}
DI unsigned ld_flag(const unsigned* p) { return __hip_atomic_load((const GAS unsigned*)p, __ATOMIC_RELAXED, __HIP_MEMORY_SCOPE_AGENT); }
DI void st_flag(unsigned* p, unsigned v) { __hip_atomic_store((GAS unsigned*)p, v, __ATOMIC_RELAXED, __HIP_MEMORY_SCOPE_AGENT); }
DI unsigned long long ld_lds64(LAS char* p) { return __hip_atomic_load((LAS unsigned long long*)p, __ATOMIC_RELAXED, __HIP_MEMORY_SCOPE_WORKGROUP); }
DI void st_lds(LAS char* p, unsigned v) { __hip_atomic_store((LAS unsigned*)p, v, __ATOMIC_RELAXED, __HIP_MEMORY_SCOPE_WORKGROUP); }
DI unsigned umin(unsigned a, unsigned b) { return a < b ? a : b; }
DI int lo_min(unsigned long long x) { return __builtin_amdgcn_readfirstlane((int)umin((unsigned)x, (unsigned)(x >> 32))); }
DI unsigned pk2(float a, float b) { typedef __attribute__((ext_vector_type(2))) __bf16 bf16x2; const bf16x2 v = {(__bf16)a, (__bf16)b}; return __builtin_bit_cast(unsigned, v); }
DI f32x4 unpk(unsigned x0, unsigned x1) { return f32x4{__uint_as_float(x0 << 16), __uint_as_float(x0 & 0xffff0000u), __uint_as_float(x1 << 16), __uint_as_float(x1 & 0xffff0000u)}; }
DI uint32_t ring_next(uint32_t x) { return x == (NRING - 1) * SLOT ? 0 : x + SLOT; }

__global__ void __launch_bounds__(384)
attn_tree(const char* __restrict__ q, const char* __restrict__ kc, const char* __restrict__ vc,
          const char* __restrict__ kt, const char* __restrict__ vt, const char* __restrict__ mask,
          char* __restrict__ ws, unsigned short* __restrict__ out) {
  extern __shared__ __attribute__((aligned(1024))) char smem_[];
  LAS char* const smem = (LAS char*)smem_;
  const int tid = threadIdx.x, lane = tid & 63;
  const int w = __builtin_amdgcn_readfirstlane(tid >> 6) - 2;
  const int bid = blockIdx.x;
  const int grp = (bid & 7) * 8 + (bid >> 5), split = (bid >> 3) & 3;
  const uint32_t lds0 = (uint32_t)(uintptr_t)smem;
  const int hi = lane >> 5, pos = lane & 31;
  const size_t kvoff = ((size_t)grp * CC + (size_t)split * (CC / NSPLIT)) * 512;
  uint32_t ko[8];
#pragma unroll
  for (int j = 0; j < 8; ++j) { ko[j] = (j >> 2) * 4096 + hi * 512 + ((pos ^ (2 * j + hi)) << 4); PIN(ko[j]); }
  st_lds(smem + SY_OFF + (w & 7) * 4, 0u);
  st_lds(smem + SY_OFF + 16 + (w & 1) * 4, 0u);
  asm volatile("" ::"s"(q), "s"(kc), "s"(vc), "s"(kt), "s"(vt), "s"(mask), "s"(ws), "s"(out));
  const char* kb = kc + kvoff + (w & 1) * 8192;
  {
    const char* src = w >= 0 ? q + ((size_t)grp * 64 + 16 * w) * 512 : kb;
    const uint32_t dst = lds0 + (w >= 0 ? P_OFF + w * PW : (w & 1) * 8192);
    dma4(src, dst, ko[0], ko[1], ko[2], ko[3]);
    dma4(src, dst + 4096, ko[4], ko[5], ko[6], ko[7]);
  }
  if (w >= 0) {
  bar();
  const int r = lane & 15, g = lane >> 4;
  unsigned* flags = (unsigned*)(ws + WS_FLAG) + (size_t)grp * 32;
  const __amdgpu_buffer_rsrc_t rs = __builtin_amdgcn_make_buffer_rsrc(ws, 0, (int)WS_END, 0x00020000);
  uint32_t kra = r * 512 + ((g ^ r) << 4);
  uint32_t pa = P_OFF + w * PW + lane * 16;
  const int vrow = 4 * g + (r >> 2);
  uint32_t vra = vrow * 128 + (((vrow >> 1) & 3) << 5) + ((r & 3) << 3);
  uint32_t tva = TV_OFF + (vrow & 7) * 512 + ((vrow & 7) << 5) + ((r & 3) << 3);
  uint32_t mbl = (uint32_t)WS_MB + grp * MB_G + w * MB_W + MB_LANE;
  uint32_t mla = (uint32_t)WS_MB + ML_B + grp * ML_G + w * ML_W + r * 8;
  uint32_t ooff = (uint32_t)(((grp * 64 + 16 * w + r) * DD + 64 * split + 4 * g) * 2);
  uint32_t fo = (lane & 7) * 4;
  uint32_t tka = TK_OFF + (r & 7) * 512 + ((g ^ (r & 7)) << 4);
  uint32_t sya = SY_OFF;
  PIN(kra); PIN(pa); PIN(vra); PIN(tva); PIN(mbl); PIN(mla); PIN(ooff); PIN(fo); PIN(tka); PIN(sya);
  auto landed = [&]() { return ld_lds64(smem + sya + 24); };
  auto await = [&](int s) { while (lo_min(landed()) <= s) { __builtin_amdgcn_s_sleep(1); } CFENCE(); };
  wait_vm<0>();
  bf16x8 qf[8];
#pragma unroll
  for (int s = 0; s < 8; ++s) qf[s] = *(const LAS bf16x8*)(smem + ((P_OFF + w * PW + kra) ^ (64 * (s & 3))) + 256 * (s >> 2));
  if (w == 0) poll_issue(flags, fo, lds0 + SN_OFF);
  dma1(mask + (size_t)(grp >> 3) * 2048 + (w & 1) * 1024, lds0 + P_OFF + w * PW + 15 * 1024, lane * 16);

  const float ninf = -__builtin_inff();
  f32x4 s0 = {ninf, ninf, ninf, ninf}, s1 = s0, ls = {0.f, 0.f, 0.f, 0.f};
  float m = -3.0e38f;
  const __bf16 one = (__bf16)1.0f;
  bf16x8 ones = {one, one, one, one, one, one, one, one};
  PIN(ones);
  unsigned beat = 0;
  uint32_t ring = 0;
  int slot = 0;
  auto settle = [&](int nrec) {
    const float mx = vmax(vmax(vmax(vmax(s0[0], s0[1]), s0[2]), vmax(vmax(s0[3], s1[0]), s1[1])), vmax(s1[2], s1[3])) * SC;
    if (__any(!(mx <= m + SLACK))) {
      const float mn = vmax(m, __builtin_ceilf(xg_max(mx)));
      const float df = fminf(mn - m, 255.f);
      m = mn;
      ls *= __builtin_amdgcn_exp2f(-df);
      const unsigned short d = (unsigned short)((unsigned)df << 7);
      const u16x8 dv = {d, d, d, d, d, d, d, d};
#pragma unroll 1
      for (int i = 0; i < nrec; ++i) {
        LAS u16x8* pp = (LAS u16x8*)(smem + pa + i * 1024);
        *pp = __builtin_elementwise_sub_sat(*pp, dv);
      }
    }
  };
  {
    bf16x8 a[16];
    uint32_t kq = kra;
    auto rd = [&](int i) { a[i] = *(const LAS bf16x8*)(smem + ((kq ^ (64 * ((i >> 1) & 3))) + 256 * (i >> 3) + 8192 * (i & 1))); };
    await(0);
#pragma unroll
    for (int i = 0; i < 6; ++i) rd(i);
    ring = SLOT; slot = 1;
#pragma unroll 1
    for (int t = 0; t < 17; ++t) {
      float e[8];
      u32x4 pf;
      unsigned long long fv;
      bool ready;
      f32x4 n[2] = {{0.f, 0.f, 0.f, 0.f}, {0.f, 0.f, 0.f, 0.f}};
      SBAR();
#pragma unroll
      for (int i = 0; i < 16; ++i) {
        n[i & 1] = __builtin_amdgcn_mfma_f32_16x16x32_bf16(a[i], qf[i >> 1], n[i & 1], 0, 0, 0);
        SBAR();
        if (i == 4) fv = landed();
        if (i == 10) {
          ready = (int)(t == 7) | (int)(lo_min(fv) > slot);
          kq = t == 7 ? tka : ring + kra;
        }
        rd((i + 6) & 15);
        if (i == 9) st_lds(smem + sya + 4 * w, (unsigned)slot);
        if (i == 1) {
          if (t == 9) {
            const u32x2 mk = *(const LAS u32x2*)(smem + P_OFF + w * PW + 15 * 1024 + r * 64 + 16 * split + 8 * (g & 1));
            const f32x4 mv = unpk(mk[0], mk[1]);
#pragma unroll
            for (int j = 0; j < 4; ++j) { s0[j] = g < 2 ? s0[j] + (1.0f - mv[j]) * MASKR : ninf; s1[j] = ninf; }
          }
          settle(t - 1);
        }
        if (i >= 2 && i < 10) { e[i - 2] = __builtin_amdgcn_exp2f(__builtin_fmaf(i < 6 ? s0[(i - 2) & 3] : s1[(i - 2) & 3], SC, -m)); PIN(e[i - 2]); }
        if (i == 10) { pf[0] = pk2(e[0], e[1]); pf[1] = pk2(e[2], e[3]); }
        if (i == 11) { pf[2] = pk2(e[4], e[5]); pf[3] = pk2(e[6], e[7]); }
        if (i == 12) *(LAS u32x4*)(smem + pa + (t == 0 ? 16 : t - 1) * 1024) = pf;
        SBAR();
      }
      ls = __builtin_amdgcn_mfma_f32_16x16x32_bf16(ones, __builtin_bit_cast(bf16x8, pf), ls, 0, 0, 0);
      SBAR();
      s0 = n[0]; s1 = n[1];
      if (t == 6) wait_vm<0>();
      SBAR();
      if (t == 10 && w == 0 && lane == split) st_flag(flags + split, *(const LAS unsigned*)(smem + SN_OFF + lane * 4) + 1u);
      if (!ready) {
        await(slot);
#pragma unroll
        for (int i = 0; i < 6; ++i) rd(i);
      }
      if (t != 7) { ring = ring_next(ring); ++slot; }
    }
  }
  f32x4 o[4];
  bf16x4 lo[16], hi4[16];
  bf16x8 P[4];
  uint32_t vb = vra + (16 % NRING) * SLOT, pq = pa;
  auto rv = [&](int j) {
    const uint32_t ad = (vb ^ (32 * (j & 3))) + (j >> 2) * 4096;
    lo[j] = __builtin_amdgcn_ds_read_tr16_b64_v4bf16((LAS bf16x4*)(smem + ad));
    hi4[j] = __builtin_amdgcn_ds_read_tr16_b64_v4bf16((LAS bf16x4*)(smem + ad + 2048));
  };
  auto rp = [&](int i) { P[i] = *(const LAS bf16x8*)(smem + pq + i * 1024); };
  bf16x4 tv[4];
  auto rtv = [&](int blk) {
#pragma unroll
    for (int db = 0; db < 4; ++db) tv[db] = __builtin_amdgcn_ds_read_tr16_b64_v4bf16((LAS bf16x4*)(smem + (tva ^ (32 * (4 * blk + db)))));
  };
  settle(16);
  const s16x4 pt = *(const LAS s16x4*)(smem + pa + 8 * 1024);
  rtv((split + 1) & 3);
  SBAR();
  rp(0); rp(1);
#pragma unroll
  for (int j = 0; j < 6; ++j) { rv(j); SBAR(); }
  const unsigned f0 = *(const LAS unsigned*)(smem + SN_OFF + lane * 4);
  SBAR();
  {
    u32x4 pf;
#pragma unroll
    for (int i = 0; i < 2; ++i) {
      pf[i] = pk2(__builtin_amdgcn_exp2f(__builtin_fmaf(s0[2 * i], SC, -m)), __builtin_amdgcn_exp2f(__builtin_fmaf(s0[2 * i + 1], SC, -m)));
      pf[2 + i] = pk2(__builtin_amdgcn_exp2f(__builtin_fmaf(s1[2 * i], SC, -m)), __builtin_amdgcn_exp2f(__builtin_fmaf(s1[2 * i + 1], SC, -m)));
    }
    *(LAS u32x4*)(smem + pa + 16 * 1024) = pf;
    ls = __builtin_amdgcn_mfma_f32_16x16x32_bf16(ones, __builtin_bit_cast(bf16x8, pf), ls, 0, 0, 0);
  }
  auto otree = [&]() {
#pragma unroll
    for (int db = 0; db < 4; ++db) o[db] = __builtin_amdgcn_mfma_f32_16x16x16bf16_1k(__builtin_bit_cast(s16x4, tv[db]), pt, f32x4{0.f, 0.f, 0.f, 0.f}, 0, 0, 0);
  };
  otree();
  auto changed = [&](int half, unsigned v) { return __all((lane & 3) == split || (lane & 4) != half || v != f0) != 0; };
  const int mine = split * MB_S;
  u32x4 hq[4] = {{0, 0, 0, 0}, {0, 0, 0, 0}, {0, 0, 0, 0}, {0, 0, 0, 0}};
  u32x4 ea[3][2]; u32x2 st[3];
  bool rok = false;
  beat = 1 + 4096 * w;
  auto heartbeat = [&]() { ++beat; if (lane == split) st_flag(flags + split, f0 + beat); };
#pragma unroll 1
  for (int v = 0; v < 16; ++v) {
    unsigned long long fv;
    bool ready;
    SBAR();
#pragma unroll
    for (int j = 0; j < 16; ++j) {
      o[j & 3] = __builtin_amdgcn_mfma_f32_16x16x32_bf16(__builtin_shufflevector(lo[j], hi4[j], 0, 1, 2, 3, 4, 5, 6, 7), P[j >> 2], o[j & 3], 0, 0, 0);
      SBAR();
      if (j == 1) rp(2);
      if (j == 4) fv = landed();
      if (j == 5) rp(3);
      if (j == 10) {
        ready = (int)(v == 15) | (int)(lo_min(fv) > slot);
        vb = ring + vra;
        pq = pa + ((v + 1) & 3) * 4096 + (((v + 1) & 2) << 9);
        rp(0);
      }
      if (j == 14) rp(1);
      rv((j + 6) & 15);
      if (j == 9) st_lds(smem + sya + 4 * w, (unsigned)slot);
      SBAR();
    }
    if ((v & 3) == 3 && v < 12) {
      const int blk = (split + 1 + (v >> 2)) & 3;
      rtv((blk + 1) & 3);
      const u32x4 p0 = {pk2(o[0][0], o[0][1]), pk2(o[0][2], o[0][3]), pk2(o[1][0], o[1][1]), pk2(o[1][2], o[1][3])};
      const u32x4 p1 = {pk2(o[2][0], o[2][1]), pk2(o[2][2], o[2][3]), pk2(o[3][0], o[3][1]), pk2(o[3][2], o[3][3])};
      const int so = blk * MB_S + (v >> 2) * MB_K;
      if (v == 11) {
        const int s1 = ((split + 1) & 3) * MB_S, s2 = ((split + 2) & 3) * MB_S + MB_K;
        __builtin_amdgcn_raw_buffer_store_b128(hq[0], rs, mbl, s1, 16);
        __builtin_amdgcn_raw_buffer_store_b128(hq[1], rs, mbl + MB_I, s1, 16);
        __builtin_amdgcn_raw_buffer_store_b128(hq[2], rs, mbl, s2, 16);
        __builtin_amdgcn_raw_buffer_store_b128(hq[3], rs, mbl + MB_I, s2, 16);
        __builtin_amdgcn_raw_buffer_store_b128(p0, rs, mbl, so, 16);
        __builtin_amdgcn_raw_buffer_store_b128(p1, rs, mbl + MB_I, so, 16);
        if (g == 0) __builtin_amdgcn_raw_buffer_store_b64(u32x2{__float_as_uint(m), __float_as_uint(ls[0])}, rs, mla, split * ML_S, 16);
      }
      hq[0] = hq[2]; hq[1] = hq[3]; hq[2] = p0; hq[3] = p1;
      otree();
    }
    SBAR();
    if (v == 9 && w == 0) {
      wait_vm<0>();
      if (changed(0, *(const LAS unsigned*)(smem + PL_OFF + lane * 4))) st_lds(smem + sya + 20, 1u);
    }
    SBAR();
    if (v == 12) {
      wait_vm<0>();
      unsigned long long c = 0;
      if (lane == 0) c = __hip_atomic_fetch_add((LAS unsigned long long*)(smem + sya + 16), 1ull, __ATOMIC_RELAXED, __HIP_MEMORY_SCOPE_WORKGROUP);
      const unsigned c0 = __builtin_amdgcn_readfirstlane((unsigned)c);
      bool cok = __builtin_amdgcn_readfirstlane((unsigned)(c >> 32)) != 0;
      if (c0 == 3) {
        for (unsigned it = 1; !cok; ++it) {
          if ((it & 15) == 0) heartbeat();
          __builtin_amdgcn_s_sleep(1);
          cok = changed(0, ld_flag(flags + (lane & 7)));
        }
        if (lane == 4 + split) st_flag(flags + 4 + split, f0 + 1u);
      }
    }
    SBAR();
    if (!ready) {
      await(slot);
#pragma unroll
      for (int j = 0; j < 6; ++j) { rv(j); SBAR(); }
    }
    SBAR();
    if (v == 13 || (v == 5 && w == 0)) poll_issue(flags, fo, lds0 + PL_OFF + w * 256);
    SBAR();
    if (v == 14) {
      wait_vm<0>();
      rok = changed(4, *(const LAS unsigned*)(smem + PL_OFF + w * 256 + lane * 4));
#pragma unroll
      for (int k = 0; k < 3; ++k) st[k] = __builtin_amdgcn_raw_buffer_load_b64(rs, mla, ((split + 3 - k) & 3) * ML_S, 16);
#pragma unroll
      for (int k = 0; k < 6; ++k) ea[k >> 1][k & 1] = __builtin_amdgcn_raw_buffer_load_b128(rs, mbl + (k & 1) * MB_I, mine + (k >> 1) * MB_K, 16);
    }
    ring = ring_next(ring); ++slot;
  }
  if (!rok) {
    unsigned it = 0;
    do { if ((++it & 15) == 0) heartbeat(); } while (!changed(4, ld_flag(flags + (lane & 7))));
    u32x4 eb[3][2]; u32x2 sb[3];
#pragma unroll
    for (int k = 0; k < 3; ++k) sb[k] = __builtin_amdgcn_raw_buffer_load_b64(rs, mla, ((split + 3 - k) & 3) * ML_S, 16);
#pragma unroll
    for (int k = 0; k < 6; ++k) eb[k >> 1][k & 1] = __builtin_amdgcn_raw_buffer_load_b128(rs, mbl + (k & 1) * MB_I, mine + (k >> 1) * MB_K, 16);
#pragma unroll
    for (int k = 0; k < 3; ++k) st[k] = sb[k];
#pragma unroll
    for (int k = 0; k < 6; ++k) ea[k >> 1][k & 1] = eb[k >> 1][k & 1];
  }
  {
    float M = m;
#pragma unroll
    for (int k = 0; k < 3; ++k) M = vmax(M, __uint_as_float(st[k][0]));
    float w0 = __builtin_amdgcn_exp2f(m - M);
    float L = w0 * ls[0], wt[3];
#pragma unroll
    for (int k = 0; k < 3; ++k) { wt[k] = __builtin_amdgcn_exp2f(__uint_as_float(st[k][0]) - M); L += wt[k] * __uint_as_float(st[k][1]); }
    const float inv = 1.0f / L;
    w0 *= inv; wt[0] *= inv; wt[1] *= inv; wt[2] *= inv;
    char* orow = (char*)out + ooff;
#pragma unroll
    for (int i = 0; i < 4; ++i) {
      const int c = i >> 1, e = 2 * (i & 1);
      const f32x4 v = o[i] * w0 + unpk(ea[0][c][e], ea[0][c][e + 1]) * wt[0] + unpk(ea[1][c][e], ea[1][c][e + 1]) * wt[1] + unpk(ea[2][c][e], ea[2][c][e + 1]) * wt[2];
      *(u32x2*)(orow + 32 * i) = u32x2{pk2(v[0], v[1]), pk2(v[2], v[3])};
    }
  }
  return;
  }
  {
    const int h = w & 1;
    uint32_t vo[4];
    const char* vb = vc + kvoff + h * 32768;
    uint32_t dst = lds0 + SLOT + h * 8192;
    auto issue = [&](int s) {
      if (s < 16) {
        const char* src = kb + (size_t)s * SLOT;
        dma4(src, dst, ko[0], ko[1], ko[2], ko[3]);
        dma4(src, dst + 4096, ko[4], ko[5], ko[6], ko[7]);
      } else {
        const char* src = vb + (size_t)(s & 3) * 65536 + ((split + 1 + (s >> 2)) & 3) * 128;
        dma4(src, dst, vo[0], vo[1], vo[2], vo[3]);
        dma4(src + 16384, dst + 4096, vo[0], vo[1], vo[2], vo[3]);
      }
      dst = dst + SLOT >= lds0 + NRING * SLOT ? dst - (NRING - 1) * SLOT : dst + SLOT;
    };
    issue(1);
    {
#pragma unroll
      for (int j = 0; j < 4; ++j) vo[j] = hi * 512 + ((pos ^ ((2 * j + hi) << h)) << 4);
      dma4((h ? vt : kt) + ((size_t)grp * 32 + 8 * split) * 512, lds0 + TK_OFF + h * 4096, vo[0], vo[1], vo[2], vo[3]);
    }
    bar();
#pragma unroll
    for (int j = 0; j < 4; ++j) vo[j] = (lane >> 3) * 512 + (((lane & 7) ^ (((lane >> 4) & 3) << 1)) << 4) + j * 3072;
    LAS char* full = smem + SY_OFF + 24 + 4 * h;
    auto min4 = [&](unsigned long long x, unsigned long long y) { return __builtin_amdgcn_readfirstlane((int)umin(umin((unsigned)x, (unsigned)(x >> 32)), umin((unsigned)y, (unsigned)(y >> 32)))); };
#pragma unroll 1
    for (int s = 0; s < 32 - 2; ++s) {
      const unsigned long long x = ld_lds64(smem + SY_OFF), y = ld_lds64(smem + SY_OFF + 8);
      SBAR();
      if (s < 2) wait_vm<8 * 2 - 4>(); else wait_vm<8 * 2 - 8>();
      st_lds(full, (unsigned)(s + 1));
      int freed = min4(x, y);
      while (freed <= s + 2 - NRING) {
        freed = min4(ld_lds64(smem + SY_OFF), ld_lds64(smem + SY_OFF + 8));
      }
      issue(s + 2);
    }
    wait_vm<8>();  st_lds(full, 31u);
    wait_vm<0>();  st_lds(full, 32u);
  }
}
}

extern "C" void kernel_launch(void* const* d_in, const int* in_sizes, int n_in, void* d_out, int out_size, void* d_ws,
                              size_t ws_size, hipStream_t stream) {
  (void)hipFuncSetAttribute((const void*)attn_tree, hipFuncAttributeMaxDynamicSharedMemorySize, LDS_BYTES);
  attn_tree<<<NGROUP * NSPLIT, 384, LDS_BYTES, stream>>>((const char*)d_in[0], (const char*)d_in[1], (const char*)d_in[2],
                                                 (const char*)d_in[3], (const char*)d_in[4], (const char*)d_in[5],
                                                 (char*)d_ws, (unsigned short*)d_out);
}
```

```cpp
#include <hip/hip_runtime.h>
#include <hip/hip_bf16.h>
#include <stdint.h>

namespace {
constexpr int NB = 8, HKV = 8, CC = 2048, DD = 256;
constexpr int NSPLIT = 4;
constexpr int NGROUP = NB * HKV;
constexpr int SLOT = 16384;
constexpr int NRING = 5;
constexpr int TK_OFF = NRING * SLOT;
constexpr int TV_OFF = TK_OFF + 4096;
constexpr int P_OFF = TV_OFF + 4096;
constexpr int PW = 16 * 1024;
constexpr int SY_OFF = P_OFF + 4 * PW;
constexpr int S_FREE = 0;
constexpr int S_DRAIN = 16, S_CHECKED = 20;
constexpr int S_RV = 24;
constexpr int S_FULL = 32;
constexpr int S_GO = 40, S_TREE = 44;
constexpr int LDS_BYTES = SY_OFF + 64;
constexpr float LOG2E = 1.4426950408889634f;
constexpr float SC = 0.0625f * LOG2E;
constexpr float MASKR = -1.0e9f * 16.0f;
constexpr float SLACK = 10.0f;
constexpr int T_OFF = 70 * 16, T_SLOT = 884, T_HOLE = 100 * 16;
constexpr int WS_FLAG = 0;
constexpr int WS_MB = 65536;
constexpr int MB_K = 2 << 20;
constexpr int WS_ML = WS_MB + 3 * MB_K;
constexpr int WS_END = WS_ML + 64 * 2048;

typedef __attribute__((ext_vector_type(8))) __bf16 bf16x8;
typedef __attribute__((ext_vector_type(4))) __bf16 bf16x4;
typedef __attribute__((ext_vector_type(4))) short s16x4;
typedef __attribute__((ext_vector_type(4))) unsigned short u16x4;
typedef __attribute__((ext_vector_type(8))) unsigned short u16x8;
typedef __attribute__((ext_vector_type(4))) float f32x4;
typedef __attribute__((ext_vector_type(4))) unsigned u32x4;
typedef __attribute__((ext_vector_type(2))) unsigned u32x2;
typedef unsigned long long u64;
#define LAS __attribute__((address_space(3)))
#define GAS __attribute__((address_space(1)))
#define DI __device__ __forceinline__
#define SBAR() __builtin_amdgcn_sched_barrier(0)
#define PIN(x) asm volatile("" : "+v"(x))
#define SPIN(x) asm volatile("" : "+s"(x))
#define CFENCE() asm volatile("" ::: "memory")
#define LP(T, a) ((LAS T*)(uintptr_t)(a))
#define MFMA(a, b, c) __builtin_amdgcn_mfma_f32_16x16x32_bf16(a, b, c, 0, 0, 0)
#define MFMA16(a, b, c) __builtin_amdgcn_mfma_f32_16x16x16bf16_1k(a, b, c, 0, 0, 0)

DI void dma4(const char* sbase, uint32_t m0v, uint32_t v0, uint32_t v1, uint32_t v2, uint32_t v3) {
  uint32_t keep;
  asm volatile(
      "s_mov_b32 %0, m0\n\ts_mov_b32 m0, %6\n\ts_nop 0\n\t"
      "global_load_lds_dwordx4 %1, %5 nt\n\t"
      "global_load_lds_dwordx4 %2, %5 offset:1024 nt\n\t"
      "global_load_lds_dwordx4 %3, %5 offset:2048 nt\n\t"
      "global_load_lds_dwordx4 %4, %5 offset:3072 nt\n\t"
      "s_mov_b32 m0, %0"
      : "=&s"(keep) : "v"(v0), "v"(v1), "v"(v2), "v"(v3), "s"(sbase), "s"(m0v) : "memory");
}
template <int N> DI void wait_vm() { asm volatile("s_waitcnt vmcnt(%0)" ::"i"(N) : "memory"); }
DI void bar() { asm volatile("s_waitcnt lgkmcnt(0)\n\ts_barrier" ::: "memory"); }
DI float vmax(float a, float b) { return __builtin_elementwise_maximum(a, b); }
DI float ex2(float x) { return __builtin_amdgcn_exp2f(x); }
DI float xg_max(float x) {
  auto s = __builtin_amdgcn_permlane16_swap(__float_as_uint(x), __float_as_uint(x), false, false);
  x = vmax(__uint_as_float(s[0]), __uint_as_float(s[1]));
  auto t = __builtin_amdgcn_permlane32_swap(__float_as_uint(x), __float_as_uint(x), false, false);
  return vmax(__uint_as_float(t[0]), __uint_as_float(t[1]));
}
DI float max8(const f32x4& a, const f32x4& b) { return vmax(vmax(vmax(vmax(a[0], a[1]), a[2]), vmax(vmax(a[3], b[0]), b[1])), vmax(b[2], b[3])); }
DI unsigned ld_flag(const unsigned* p) { return __hip_atomic_load((const GAS unsigned*)p, __ATOMIC_RELAXED, __HIP_MEMORY_SCOPE_AGENT); }
DI void st_flag(unsigned* p, unsigned v) { __hip_atomic_store((GAS unsigned*)p, v, __ATOMIC_RELAXED, __HIP_MEMORY_SCOPE_AGENT); }
DI unsigned ld_lds(uint32_t a) { return __hip_atomic_load(LP(unsigned, a), __ATOMIC_RELAXED, __HIP_MEMORY_SCOPE_WORKGROUP); }
DI u32x4 ld_lds128(uint32_t a) { return *LP(volatile u32x4, a); }
DI void st_lds(uint32_t a, unsigned v) { __hip_atomic_store(LP(unsigned, a), v, __ATOMIC_RELAXED, __HIP_MEMORY_SCOPE_WORKGROUP); }
DI int rfl(unsigned x) { return __builtin_amdgcn_readfirstlane((int)x); }
DI u32x2 ld_lds64(uint32_t a) { return *LP(volatile u32x2, a); }
template <class T> DI int landed(const T& x) { return rfl(x[0] < x[1] ? x[0] : x[1]); }
DI unsigned pk2(float a, float b) { typedef __attribute__((ext_vector_type(2))) __bf16 bf16x2; const bf16x2 v = {(__bf16)a, (__bf16)b}; return __builtin_bit_cast(unsigned, v); }
DI f32x4 unpk(unsigned x0, unsigned x1) { return f32x4{__uint_as_float(x0 << 16), __uint_as_float(x0 & 0xffff0000u), __uint_as_float(x1 << 16), __uint_as_float(x1 & 0xffff0000u)}; }
DI uint32_t ring_next(uint32_t x) { return x == (NRING - 1) * SLOT ? 0 : x + SLOT; }

__global__ void __launch_bounds__(448)
attn_tree(const char* __restrict__ q, const char* __restrict__ kc, const char* __restrict__ vc,
          const char* __restrict__ kt, const char* __restrict__ vt, const char* __restrict__ mask,
          char* __restrict__ ws, unsigned short* __restrict__ out) {
  extern __shared__ __attribute__((aligned(1024))) char smem_[];
  const int tid = threadIdx.x, lane = tid & 63;
  const int w = rfl(tid >> 6) - 3;
  const int bid = blockIdx.x;
  const int grp = (bid & 7) * 8 + (bid >> 5), split = (bid >> 3) & 3;
  const uint32_t lds0 = (uint32_t)(uintptr_t)(LAS char*)smem_;
  const int hi = lane >> 5, pos = lane & 31;
  const size_t kvoff = ((size_t)grp * CC + (size_t)split * (CC / NSPLIT)) * 512;
  const size_t troff = ((size_t)grp * 32 + 8 * split) * 512;
  uint32_t ko[8];
#pragma unroll
  for (int j = 0; j < 8; ++j) { ko[j] = (j >> 2) * 4096 + hi * 512 + ((pos ^ (2 * j + hi)) << 4); PIN(ko[j]); }
  uint32_t sya = lds0 + SY_OFF;
  PIN(sya);
  st_lds(sya + (lane & 15) * 4, 0u);
  asm volatile("" ::"s"(q), "s"(kc), "s"(vc), "s"(kt), "s"(vt), "s"(mask), "s"(ws), "s"(out));
  u64 t64 = __builtin_amdgcn_s_memrealtime();
  const int h = ~w & 1;
  const char* kb = kc + kvoff + h * 8192;
  if (w != -1) {
    const char* src = w >= 0 ? q + ((size_t)grp * 64 + 16 * w) * 512 : kb;
    const uint32_t dst = lds0 + (w >= 0 ? P_OFF + w * PW : h * 8192);
    dma4(src, dst, ko[0], ko[1], ko[2], ko[3]);
    dma4(src, dst + 4096, ko[4], ko[5], ko[6], ko[7]);
  }
  unsigned* flags = (unsigned*)(ws + WS_FLAG) + (size_t)grp * 32;
  if (w >= 0) {
  const int r = lane & 15, g = lane >> 4;
  bar();
  const u32x2 mk = *(const u32x2*)(mask + (((grp >> 3) * 32 + 16 * (w & 1) + r) * 32 + 8 * split + 4 * (g & 1)) * 2);
  SBAR();
  const __amdgpu_buffer_rsrc_t rs = __builtin_amdgcn_make_buffer_rsrc(ws, 0, WS_END, 0x00020000);
  uint32_t ka[4], va[4];
  const int vrow = 4 * g + (r >> 2);
#pragma unroll
  for (int j = 0; j < 4; ++j) {
    ka[j] = lds0 + ((r * 512 + ((g ^ r) << 4)) ^ (64 * j));
    va[j] = lds0 + ((vrow * 128 + (((vrow >> 1) & 3) << 5) + ((r & 3) << 3)) ^ (32 * j));
    PIN(ka[j]); PIN(va[j]);
  }
  uint32_t pa = lds0 + P_OFF + w * PW + lane * 16;
  uint32_t tva = lds0 + TV_OFF + (vrow & 7) * 512 + ((vrow & 7) << 5) + ((r & 3) << 3);
  uint32_t mbl = WS_MB + grp * 32768 + w * 2048 + lane * 16;
  uint32_t mla = WS_ML + grp * 2048 + (16 * w + r) * 8;
  const __amdgpu_buffer_rsrc_t ro = __builtin_amdgcn_make_buffer_rsrc(out, 0, NGROUP * 64 * DD * 2, 0x00020000);
  uint32_t ooff = (uint32_t)(((grp * 64 + 16 * w + r) * DD + 64 * split + 16 * (g & 1) + 8 * (g >> 1)) * 2);
  uint32_t tka = lds0 + TK_OFF + (r & 7) * 512 + ((g ^ (r & 7)) << 4);
  uint32_t fra = sya + S_FREE + 4 * w;
  PIN(pa); PIN(tva); PIN(mbl); PIN(mla); PIN(ooff); PIN(tka); PIN(fra);
  const __bf16 one = (__bf16)1.0f;
  bf16x8 ones = {one, one, one, one, one, one, one, one};
  PIN(ones);
  wait_vm<1>();
  bf16x8 qf[8];
#pragma unroll
  for (int s = 0; s < 8; ++s) qf[s] = *LP(const bf16x8, ka[s & 3] + (P_OFF + w * PW) + 256 * (s >> 2));
  int have;
  for (;;) {
    const u32x4 x = ld_lds128(sya + S_FULL);
    have = landed(x);
    if (rfl(x[3])) break;
    __builtin_amdgcn_s_sleep(1);
  }
  CFENCE();
  const f32x4 zero = {0.f, 0.f, 0.f, 0.f};
  float m;
  f32x4 ls;
  u32x2 pt;
  {
    bf16x8 a[8];
#pragma unroll
    for (int s = 0; s < 8; ++s) a[s] = *LP(const bf16x8, (tka ^ (64 * (s & 3))) + 256 * (s >> 2));
    f32x4 n = zero;
#pragma unroll
    for (int s = 0; s < 8; ++s) n = MFMA(a[s], qf[s], n);
    const f32x4 mv = unpk(mk[0], mk[1]);
#pragma unroll
    for (int j = 0; j < 4; ++j) n[j] = g < 2 ? n[j] + (1.0f - mv[j]) * MASKR : -__builtin_inff();
    m = vmax(-3.0e38f, __builtin_ceilf(xg_max(vmax(vmax(n[0], n[1]), vmax(n[2], n[3])) * SC)));
    pt = u32x2{pk2(ex2(__builtin_fmaf(n[0], SC, -m)), ex2(__builtin_fmaf(n[1], SC, -m))), pk2(ex2(__builtin_fmaf(n[2], SC, -m)), ex2(__builtin_fmaf(n[3], SC, -m)))};
    ls = MFMA(ones, __builtin_bit_cast(bf16x8, u32x4{pt[0], pt[1], 0u, 0u}), zero);
  }
  uint32_t ring = 0;
  uint32_t pw = pa;
#pragma unroll 1
  for (int t = 0; t < 16; ++t) {
    uint32_t kq[4];
#pragma unroll
    for (int j = 0; j < 4; ++j) { kq[j] = ka[j] + ring; PIN(kq[j]); }
    unsigned nx = t + 1;
    PIN(nx);
    ring = ring_next(ring);
    while (have <= t) {
      have = landed(ld_lds64(sya + S_FULL));
      if (have > t) break;
      __builtin_amdgcn_s_sleep(1);
    }
    CFENCE();
    SBAR();
    bf16x8 a[16];
#pragma unroll
    for (int i = 0; i < 16; ++i) { a[i] = *LP(const bf16x8, kq[(i >> 1) & 3] + 256 * (i >> 3) + 8192 * (i & 1)); SBAR(); }
    st_lds(fra, nx);
    SBAR();
    f32x4 n[2] = {zero, zero};
    u32x2 fv;
#pragma unroll
    for (int i = 0; i < 16; ++i) {
      n[i & 1] = MFMA(a[i], qf[i >> 1], n[i & 1]);
      SBAR();
      if (i == 12) fv = ld_lds64(sya + S_FULL);
      SBAR();
    }
    f32x4 e0, e1;
#pragma unroll
    for (int j = 0; j < 4; ++j) { e0[j] = __builtin_fmaf(n[0][j], SC, -m); PIN(e0[j]); }
#pragma unroll
    for (int j = 0; j < 4; ++j) { e1[j] = __builtin_fmaf(n[1][j], SC, -m); PIN(e1[j]); }
    if (__any(!(max8(e0, e1) <= SLACK))) {
      const float mn = vmax(m, __builtin_ceilf(xg_max(max8(n[0], n[1]) * SC)));
      const float df = fminf(mn - m, 255.f);
      m = mn;
      ls *= ex2(-df);
      const unsigned short d = (unsigned short)((unsigned)df << 7);
      pt = __builtin_bit_cast(u32x2, __builtin_elementwise_sub_sat(__builtin_bit_cast(u16x4, pt), u16x4{d, d, d, d}));
      const u16x8 dv = {d, d, d, d, d, d, d, d};
#pragma unroll 1
      for (int i = 0; i < t; ++i) {
        LAS u16x8* pp = LP(u16x8, pa + i * 1024);
        *pp = __builtin_elementwise_sub_sat(*pp, dv);
      }
#pragma unroll
      for (int j = 0; j < 4; ++j) { e0[j] = __builtin_fmaf(n[0][j], SC, -m); e1[j] = __builtin_fmaf(n[1][j], SC, -m); }
    }
    const u32x4 pf = {pk2(ex2(e0[0]), ex2(e0[1])), pk2(ex2(e0[2]), ex2(e0[3])), pk2(ex2(e1[0]), ex2(e1[1])), pk2(ex2(e1[2]), ex2(e1[3]))};
    *LP(u32x4, pw) = pf;
    pw += 1024;
    ls = MFMA(ones, __builtin_bit_cast(bf16x8, pf), ls);
    have = landed(fv);
  }
  f32x4 o[4];
  u32x4 e[6];
  u32x4 x0 = {0u, 0u, 0u, 0u}, x1 = x0, x2 = x0, x3 = x0;
  u32x2 st[3];
#pragma unroll
  for (int k = 0; k < 6; ++k) e[k] = u32x4{0u, 0u, 0u, 0u};
#pragma unroll
  for (int k = 0; k < 3; ++k) st[k] = u32x2{0u, 0u};
  bf16x4 tv[4];
  auto rtv = [&](int blk) {
#pragma unroll
    for (int db = 0; db < 4; ++db) tv[db] = __builtin_amdgcn_ds_read_tr16_b64_v4bf16(LP(bf16x4, tva ^ (32 * (4 * blk + db))));
    SBAR();
  };
  auto otree = [&]() {
    SBAR();
#pragma unroll
    for (int db = 0; db < 4; ++db) o[db] = MFMA16(__builtin_bit_cast(s16x4, tv[db]), __builtin_bit_cast(s16x4, pt), zero);
  };
  uint32_t vq[4];
  bf16x8 P[4];
  unsigned nx;
  int s = 16;
  auto prep = [&]() {
    const uint32_t pq = pa + (s & 3) * 4096;
#pragma unroll
    for (int i = 0; i < 4; ++i) P[i] = *LP(const bf16x8, pq + i * 1024);
#pragma unroll
    for (int j = 0; j < 4; ++j) { vq[j] = va[j] + ring; PIN(vq[j]); }
    nx = s + 1;
    PIN(nx);
    ring = ring_next(ring);
    SBAR();
  };
  while (rfl(ld_lds(sya + S_TREE)) != 2) { }
  CFENCE();
  rtv((split + 1) & 3);
  prep();
  otree();
  int gw = 0, ign = 1, left = 17;
  auto signal = [&]() {
    wait_vm<0>();
    u64 x = 0;
    if (lane == 0) x = __hip_atomic_fetch_add(LP(u64, sya + S_DRAIN), 1ull, __ATOMIC_RELAXED, __HIP_MEMORY_SCOPE_WORKGROUP);
    CFENCE();
    unsigned rv_ = ld_lds(sya + S_RV);
    if (rfl((unsigned)x) == 3) {
      unsigned cok = rfl((unsigned)(x >> 32));
      while (!cok) { __builtin_amdgcn_s_sleep(1); cok = rfl(ld_lds(sya + S_CHECKED)); CFENCE(); rv_ = ld_lds(sya + S_RV); }
      if (lane == 0) st_flag(flags + 4 + split, rv_);
    }
  };
#pragma unroll 1
  do {
    while (have <= s && gw <= ign) {
      const u32x4 x = ld_lds128(sya + S_FULL);
      have = landed(x);
      gw = rfl(x[2]);
      if (have > s) break;
      __builtin_amdgcn_s_sleep(1);
    }
    CFENCE();
    SPIN(gw);
    if (gw > ign) {
#pragma unroll
      for (int k = 0; k < 3; ++k) st[k] = __builtin_amdgcn_raw_buffer_load_b64(rs, mla, ((split + 3 - k) & 3) * 512, 16);
#pragma unroll
      for (int k = 0; k < 6; ++k) e[k] = __builtin_amdgcn_raw_buffer_load_b128(rs, mbl + (k & 1) * 1024, split * 8192 + (k >> 1) * MB_K, 16);
      ign = 1;
      --left;
    }
    SPIN(have);
    SBAR();
    if (have > s) {
      bf16x4 lo[16], hi4[16];
      auto rv = [&](int j) {
        lo[j] = __builtin_amdgcn_ds_read_tr16_b64_v4bf16(LP(bf16x4, vq[j & 3] + (j >> 2) * 4096));
        hi4[j] = __builtin_amdgcn_ds_read_tr16_b64_v4bf16(LP(bf16x4, vq[j & 3] + (j >> 2) * 4096 + 2048));
      };
#pragma unroll
      for (int j = 0; j < 7; ++j) { rv(j); SBAR(); }
      u32x4 fv;
#pragma unroll
      for (int j = 0; j < 16; ++j) {
        o[j & 3] = MFMA(__builtin_shufflevector(lo[j], hi4[j], 0, 1, 2, 3, 4, 5, 6, 7), P[j >> 2], o[j & 3]);
        SBAR();
        if (j < 9) rv(j + 7);
        if (j == 8) st_lds(fra, nx);
        if (j == 10) fv = ld_lds128(sya + S_FULL);
        SBAR();
      }
      have = landed(fv);
      gw = rfl(fv[2]);
      int ev = (0x1c880000u >> s) & 1;
      --left;
      ++s;
      SPIN(ev);
      if (ev) {
        int c = s;
        SPIN(c);
        auto put = [&](int k, const u32x4& a, const u32x4& b) {
          __builtin_amdgcn_raw_buffer_store_b128(a, rs, mbl, ((split + 1 + k) & 3) * 8192 + k * MB_K, 16);
          __builtin_amdgcn_raw_buffer_store_b128(b, rs, mbl + 1024, ((split + 1 + k) & 3) * 8192 + k * MB_K, 16);
        };
        if ((c & 3) == 0) {
          rtv((split + (s >> 2) - 3) & 3);
          x0 = x2; x1 = x3;
          PIN(x0); PIN(x1);
          SBAR();
          x2 = u32x4{pk2(o[0][0], o[0][1]), pk2(o[0][2], o[0][3]), pk2(o[1][0], o[1][1]), pk2(o[1][2], o[1][3])};
          x3 = u32x4{pk2(o[2][0], o[2][1]), pk2(o[2][2], o[2][3]), pk2(o[3][0], o[3][1]), pk2(o[3][2], o[3][3])};
          int d = c;
          SPIN(d);
          if (d == 28) put(2, x2, x3);
          otree();
        }
        SPIN(c);
        if (c == 27) {
          put(0, x0, x1);
          if (g == 0) __builtin_amdgcn_raw_buffer_store_b64(u32x2{__float_as_uint(m), __float_as_uint(ls[0])}, rs, mla, split * 512, 16);
          put(1, x2, x3);
        }
        SPIN(c);
        if (c == 29) { signal(); ign = 0; }
      }
      prep();
    }
  } while (left);
  {
    float M = m;
#pragma unroll
    for (int k = 0; k < 3; ++k) M = vmax(M, __uint_as_float(st[k][0]));
    float w0 = ex2(m - M);
    float L = w0 * ls[0], wt[3];
#pragma unroll
    for (int k = 0; k < 3; ++k) { wt[k] = ex2(__uint_as_float(st[k][0]) - M); L += wt[k] * __uint_as_float(st[k][1]); }
    const float inv = 1.0f / L;
    w0 *= inv; wt[0] *= inv; wt[1] *= inv; wt[2] *= inv;
    u32x2 ov[4];
#pragma unroll
    for (int i = 0; i < 4; ++i) {
      const int c = i >> 1, x = 2 * (i & 1);
      const f32x4 v = o[i] * w0 + unpk(e[c][x], e[c][x + 1]) * wt[0] + unpk(e[2 + c][x], e[2 + c][x + 1]) * wt[1] + unpk(e[4 + c][x], e[4 + c][x + 1]) * wt[2];
      ov[i] = u32x2{pk2(v[0], v[1]), pk2(v[2], v[3])};
      if (i & 1) {
        const auto a = __builtin_amdgcn_permlane16_swap(ov[i - 1][0], ov[i][0], false, false);
        const auto b = __builtin_amdgcn_permlane16_swap(ov[i - 1][1], ov[i][1], false, false);
        __builtin_amdgcn_raw_buffer_store_b128(u32x4{a[0], b[0], a[1], b[1]}, ro, ooff + 64 * c, 0, 16);
      }
    }
  }
  return;
  }
  if (w == -1) {
    bar();
    dma4(kt + troff, lds0 + TK_OFF, ko[0], ko[1], ko[2], ko[3]);
    uint32_t vo[4];
#pragma unroll
    for (int j = 0; j < 4; ++j) vo[j] = hi * 512 + ((pos ^ ((2 * j + hi) << 1)) << 4);
    unsigned* fp = flags + (lane & 7);
    wait_vm<0>();
    st_lds(sya + S_TREE, 1u);
    dma4(vt + troff, lds0 + TV_OFF, vo[0], vo[1], vo[2], vo[3]);
    const unsigned f0 = ld_flag(fp);
    wait_vm<0>();
    st_lds(sya + S_TREE, 2u);
    if (lane == 4 + split) st_lds(sya + S_RV, f0 + 1u);
    unsigned beat = 0;
    auto upto = [&](int n) { while (landed(ld_lds64(sya + S_FULL)) <= n) { __builtin_amdgcn_s_sleep(4); } };
    auto bump = [&]() { ++beat; if (lane == split) st_flag(fp, f0 + beat); };
    auto changed = [&](int half, unsigned v) { return __all((lane & 3) == split || (lane & 4) != half || v != f0) != 0; };
    upto(6);
    for (unsigned it = 0; ; ++it) {
      if ((it & 15) == 0) bump();
      __builtin_amdgcn_s_sleep(32);
      if (changed(0, ld_flag(fp))) break;
    }
    st_lds(sya + S_CHECKED, 1u);
    upto(30);
    for (unsigned it = 1; !changed(4, ld_flag(fp)); ++it) {
      if ((it & 15) == 0) bump();
    }
    st_lds(sya + S_GO, 1u);
    return;
  }
  {
    const uint32_t ld0 = lds0 + h * 8192;
    dma4(kb + SLOT, ld0 + SLOT, ko[0], ko[1], ko[2], ko[3]);
    dma4(kb + SLOT, ld0 + SLOT + 4096, ko[4], ko[5], ko[6], ko[7]);
    const char* vb = vc + kvoff + h * 32768;
    uint32_t fa = sya + S_FREE + (lane & 3) * 4, fu = sya + S_FULL + 4 * h;
    PIN(fa); PIN(fu);
    uint32_t dst = 2 * SLOT;
    SPIN(t64);
    const unsigned tstart = (unsigned)t64;
    int due = T_OFF + 2 * T_SLOT;
    auto early = [&]() { return (int)((((unsigned)__builtin_amdgcn_s_memrealtime() - tstart) & 0xffffffu) << 4) < due; };
    bar();
#pragma unroll 1
    for (int n = 2; n < 32; ++n) {
      int fw = (int)ld_lds(fa);
      if (n == 16) {
#pragma unroll
        for (int j = 0; j < 8; ++j) ko[j] = (lane >> 3) * 512 + (((lane & 7) ^ (((lane >> 4) & 3) << 1)) << 4) + (j & 3) * 3072 + (j >> 2) * 16384;
      }
      const char* src = n < 16 ? kb + (size_t)n * SLOT : vb + (size_t)(n & 3) * 65536 + ((split + 1 + (n >> 2)) & 3) * 128;
      uint32_t d0 = ld0 + dst, d1 = d0 + 4096;
      dst = ring_next(dst);
      asm volatile("" : "+s"(src), "+s"(d0), "+s"(d1), "+s"(dst));
      unsigned fl = n - 1;
      PIN(fl);
      const int need = n >= 31 ? 28 : n - NRING;
      const bool busy = __any(fw <= need);
      SBAR();
      wait_vm<16>();
      st_lds(fu, fl - 1);
      if (n < 3 || n == 31 || early()) {
        wait_vm<8>();
        st_lds(fu, fl);
      }
      if (busy) {
        do { fw = (int)ld_lds(fa); } while (__any(fw <= need));
      }
      while (early()) __builtin_amdgcn_s_sleep(1);
      due += n == 29 ? T_SLOT + T_HOLE : T_SLOT;
      dma4(src, d0, ko[0], ko[1], ko[2], ko[3]);
      dma4(src, d1, ko[4], ko[5], ko[6], ko[7]);
    }
    wait_vm<16>();  st_lds(fu, 30u);
    wait_vm<8>();  st_lds(fu, 31u);
    wait_vm<0>();  st_lds(fu, 32u);
  }
}
}

extern "C" void kernel_launch(void* const* d_in, const int* in_sizes, int n_in, void* d_out, int out_size, void* d_ws,
                              size_t ws_size, hipStream_t stream) {
  (void)hipFuncSetAttribute((const void*)attn_tree, hipFuncAttributeMaxDynamicSharedMemorySize, LDS_BYTES);
  attn_tree<<<NGROUP * NSPLIT, 448, LDS_BYTES, stream>>>((const char*)d_in[0], (const char*)d_in[1], (const char*)d_in[2],
                                                 (const char*)d_in[3], (const char*)d_in[4], (const char*)d_in[5],
                                                 (char*)d_ws, (unsigned short*)d_out);
}
```

```cpp
#include <hip/hip_runtime.h>
#include <hip/hip_bf16.h>
#include <stdint.h>

namespace {
constexpr int NB = 8, HKV = 8, CC = 2048, DD = 256;
constexpr int NSPLIT = 4;
constexpr int NGROUP = NB * HKV;
constexpr int SLOT = 16384;
constexpr int NRING = 5;
constexpr int TK_OFF = NRING * SLOT;
constexpr int TV_OFF = TK_OFF + 4096;
constexpr int P_OFF = TV_OFF + 4096;
constexpr int PW = 16 * 1024;
constexpr int SY_OFF = P_OFF + 4 * PW;
constexpr int S_FREE = 0;
constexpr int S_DRAIN = 16, S_CHECKED = 20;
constexpr int S_RV = 24;
constexpr int S_FULL = 32;
constexpr int S_GO = 40, S_TREE = 44;
constexpr int S_ADJ = 48;
constexpr int LDS_BYTES = SY_OFF + 64;
constexpr float LOG2E = 1.4426950408889634f;
constexpr float SC = 0.0625f * LOG2E;
constexpr float MASKR = -1.0e9f * 16.0f;
constexpr float SLACK = 10.0f;
constexpr int T_OFF = 45 * 16, T_K = 885, T_V2 = 884, T_V1 = 900, T_HOLE = 100 * 16;
constexpr int WS_FLAG = 0;
constexpr int WS_T0 = 16384;
constexpr int WS_MB = 65536;
constexpr int MB_K = 2 << 20;
constexpr int WS_ML = WS_MB + 3 * MB_K;
constexpr int WS_END = WS_ML + 64 * 2048;

typedef __attribute__((ext_vector_type(8))) __bf16 bf16x8;
typedef __attribute__((ext_vector_type(4))) __bf16 bf16x4;
typedef __attribute__((ext_vector_type(4))) short s16x4;
typedef __attribute__((ext_vector_type(4))) unsigned short u16x4;
typedef __attribute__((ext_vector_type(8))) unsigned short u16x8;
typedef __attribute__((ext_vector_type(4))) float f32x4;
typedef __attribute__((ext_vector_type(4))) unsigned u32x4;
typedef __attribute__((ext_vector_type(2))) unsigned u32x2;
typedef unsigned long long u64;
#define LAS __attribute__((address_space(3)))
#define GAS __attribute__((address_space(1)))
#define DI __device__ __forceinline__
#define SBAR() __builtin_amdgcn_sched_barrier(0)
#define PIN(x) asm volatile("" : "+v"(x))
#define SPIN(x) asm volatile("" : "+s"(x))
#define CFENCE() asm volatile("" ::: "memory")
#define LP(T, a) ((LAS T*)(uintptr_t)(a))
#define MFMA(a, b, c) __builtin_amdgcn_mfma_f32_16x16x32_bf16(a, b, c, 0, 0, 0)
#define MFMA16(a, b, c) __builtin_amdgcn_mfma_f32_16x16x16bf16_1k(a, b, c, 0, 0, 0)

DI void dma4(const char* sbase, uint32_t m0v, uint32_t v0, uint32_t v1, uint32_t v2, uint32_t v3) {
  uint32_t keep;
  asm volatile(
      "s_mov_b32 %0, m0\n\ts_mov_b32 m0, %6\n\ts_nop 0\n\t"
      "global_load_lds_dwordx4 %1, %5 nt\n\t"
      "global_load_lds_dwordx4 %2, %5 offset:1024 nt\n\t"
      "global_load_lds_dwordx4 %3, %5 offset:2048 nt\n\t"
      "global_load_lds_dwordx4 %4, %5 offset:3072 nt\n\t"
      "s_mov_b32 m0, %0"
      : "=&s"(keep) : "v"(v0), "v"(v1), "v"(v2), "v"(v3), "s"(sbase), "s"(m0v) : "memory");
}
template <int N> DI void wait_vm() { asm volatile("s_waitcnt vmcnt(%0)" ::"i"(N) : "memory"); }
DI void bar() { asm volatile("s_waitcnt lgkmcnt(0)\n\ts_barrier" ::: "memory"); }
DI float vmax(float a, float b) { return __builtin_elementwise_maximum(a, b); }
DI float ex2(float x) { return __builtin_amdgcn_exp2f(x); }
DI float xg_max(float x) {
  auto s = __builtin_amdgcn_permlane16_swap(__float_as_uint(x), __float_as_uint(x), false, false);
  x = vmax(__uint_as_float(s[0]), __uint_as_float(s[1]));
  auto t = __builtin_amdgcn_permlane32_swap(__float_as_uint(x), __float_as_uint(x), false, false);
  return vmax(__uint_as_float(t[0]), __uint_as_float(t[1]));
}
DI float max8(const f32x4& a, const f32x4& b) { return vmax(vmax(vmax(vmax(a[0], a[1]), a[2]), vmax(vmax(a[3], b[0]), b[1])), vmax(b[2], b[3])); }
DI unsigned ld_flag(const unsigned* p) { return __hip_atomic_load((const GAS unsigned*)p, __ATOMIC_RELAXED, __HIP_MEMORY_SCOPE_AGENT); }
DI void st_flag(unsigned* p, unsigned v) { __hip_atomic_store((GAS unsigned*)p, v, __ATOMIC_RELAXED, __HIP_MEMORY_SCOPE_AGENT); }
DI unsigned ld_lds(uint32_t a) { return __hip_atomic_load(LP(unsigned, a), __ATOMIC_RELAXED, __HIP_MEMORY_SCOPE_WORKGROUP); }
DI u32x4 ld_lds128(uint32_t a) { return *LP(volatile u32x4, a); }
DI void st_lds(uint32_t a, unsigned v) { __hip_atomic_store(LP(unsigned, a), v, __ATOMIC_RELAXED, __HIP_MEMORY_SCOPE_WORKGROUP); }
DI int rfl(unsigned x) { return __builtin_amdgcn_readfirstlane((int)x); }
DI u32x2 ld_lds64(uint32_t a) { return *LP(volatile u32x2, a); }
template <class T> DI int landed(const T& x) { return rfl(x[0] < x[1] ? x[0] : x[1]); }
DI unsigned pk2(float a, float b) { typedef __attribute__((ext_vector_type(2))) __bf16 bf16x2; const bf16x2 v = {(__bf16)a, (__bf16)b}; return __builtin_bit_cast(unsigned, v); }
DI f32x4 unpk(unsigned x0, unsigned x1) { return f32x4{__uint_as_float(x0 << 16), __uint_as_float(x0 & 0xffff0000u), __uint_as_float(x1 << 16), __uint_as_float(x1 & 0xffff0000u)}; }
DI uint32_t ring_next(uint32_t x) { return x == (NRING - 1) * SLOT ? 0 : x + SLOT; }

__global__ void __launch_bounds__(448)
attn_tree(const char* __restrict__ q, const char* __restrict__ kc, const char* __restrict__ vc,
          const char* __restrict__ kt, const char* __restrict__ vt, const char* __restrict__ mask,
          char* __restrict__ ws, unsigned short* __restrict__ out) {
  extern __shared__ __attribute__((aligned(1024))) char smem_[];
  const int tid = threadIdx.x, lane = tid & 63;
  const int w = rfl(tid >> 6) - 3;
  const int bid = blockIdx.x;
  const int grp = (bid & 7) * 8 + (bid >> 5), split = (bid >> 3) & 3;
  const uint32_t lds0 = (uint32_t)(uintptr_t)(LAS char*)smem_;
  const int hi = lane >> 5, pos = lane & 31;
  const size_t kvoff = ((size_t)grp * CC + (size_t)split * (CC / NSPLIT)) * 512;
  const size_t troff = ((size_t)grp * 32 + 8 * split) * 512;
  uint32_t ko[8];
#pragma unroll
  for (int j = 0; j < 8; ++j) { ko[j] = (j >> 2) * 4096 + hi * 512 + ((pos ^ (2 * j + hi)) << 4); PIN(ko[j]); }
  uint32_t sya = lds0 + SY_OFF;
  PIN(sya);
  st_lds(sya + (lane & 15) * 4, 0u);
  asm volatile("" ::"s"(q), "s"(kc), "s"(vc), "s"(kt), "s"(vt), "s"(mask), "s"(ws), "s"(out));
  u64 t64 = __builtin_amdgcn_s_memrealtime();
  const int h = ~w & 1;
  const char* kb = kc + kvoff + h * 8192;
  if (w != -1) {
    const char* src = w >= 0 ? q + ((size_t)grp * 64 + 16 * w) * 512 : kb;
    const uint32_t dst = lds0 + (w >= 0 ? P_OFF + w * PW : h * 8192);
    dma4(src, dst, ko[0], ko[1], ko[2], ko[3]);
    dma4(src, dst + 4096, ko[4], ko[5], ko[6], ko[7]);
  }
  unsigned* flags = (unsigned*)(ws + WS_FLAG) + (size_t)grp * 32;
  if (w >= 0) {
  const int r = lane & 15, g = lane >> 4;
  const u32x2 mk = *(const u32x2*)(mask + (((grp >> 3) * 32 + 16 * (w & 1) + r) * 32 + 8 * split + 4 * (g & 1)) * 2);
  SBAR();
  bar();
  const __amdgpu_buffer_rsrc_t rs = __builtin_amdgcn_make_buffer_rsrc(ws, 0, WS_END, 0x00020000);
  uint32_t ka[4], va[4];
  const int vrow = 4 * g + (r >> 2);
#pragma unroll
  for (int j = 0; j < 4; ++j) {
    ka[j] = lds0 + ((r * 512 + ((g ^ r) << 4)) ^ (64 * j));
    va[j] = lds0 + ((vrow * 256 + ((vrow & 7) << 5) + ((r & 3) << 3)) ^ (32 * j));
    PIN(ka[j]); PIN(va[j]);
  }
  uint32_t pa = lds0 + P_OFF + w * PW + lane * 16;
  uint32_t tva = lds0 + TV_OFF + (vrow & 7) * 512 + ((vrow & 7) << 5) + ((r & 3) << 3);
  uint32_t mbl = WS_MB + grp * 32768 + w * 2048 + lane * 16;
  uint32_t mla = WS_ML + grp * 2048 + (16 * w + r) * 8;
  const __amdgpu_buffer_rsrc_t ro = __builtin_amdgcn_make_buffer_rsrc(out, 0, NGROUP * 64 * DD * 2, 0x00020000);
  uint32_t ooff = (uint32_t)(((grp * 64 + 16 * w + r) * DD + 64 * split + 16 * (g & 1) + 8 * (g >> 1)) * 2);
  uint32_t tka = lds0 + TK_OFF + (r & 7) * 512 + ((g ^ (r & 7)) << 4);
  uint32_t fra = sya + S_FREE + 4 * w;
  PIN(pa); PIN(tva); PIN(mbl); PIN(mla); PIN(ooff); PIN(tka); PIN(fra);
  const __bf16 one = (__bf16)1.0f;
  bf16x8 ones = {one, one, one, one, one, one, one, one};
  PIN(ones);
  wait_vm<1>();
  bf16x8 qf[8];
#pragma unroll
  for (int s = 0; s < 8; ++s) qf[s] = *LP(const bf16x8, ka[s & 3] + (P_OFF + w * PW) + 256 * (s >> 2));
  int have;
  for (;;) {
    const u32x4 x = ld_lds128(sya + S_FULL);
    have = landed(x);
    if (rfl(x[3])) break;
    __builtin_amdgcn_s_sleep(1);
  }
  CFENCE();
  const f32x4 zero = {0.f, 0.f, 0.f, 0.f};
  float m;
  f32x4 ls;
  u32x2 pt;
  {
    bf16x8 a[8];
#pragma unroll
    for (int s = 0; s < 8; ++s) a[s] = *LP(const bf16x8, (tka ^ (64 * (s & 3))) + 256 * (s >> 2));
    f32x4 n = zero;
#pragma unroll
    for (int s = 0; s < 8; ++s) n = MFMA(a[s], qf[s], n);
    const f32x4 mv = unpk(mk[0], mk[1]);
#pragma unroll
    for (int j = 0; j < 4; ++j) n[j] = g < 2 ? n[j] + (1.0f - mv[j]) * MASKR : -__builtin_inff();
    m = vmax(-3.0e38f, __builtin_ceilf(xg_max(vmax(vmax(n[0], n[1]), vmax(n[2], n[3])) * SC)));
    pt = u32x2{pk2(ex2(__builtin_fmaf(n[0], SC, -m)), ex2(__builtin_fmaf(n[1], SC, -m))), pk2(ex2(__builtin_fmaf(n[2], SC, -m)), ex2(__builtin_fmaf(n[3], SC, -m)))};
    ls = MFMA(ones, __builtin_bit_cast(bf16x8, u32x4{pt[0], pt[1], 0u, 0u}), zero);
  }
  uint32_t ring = 0;
  uint32_t pw = pa;
#pragma unroll 1
  for (int t = 0; t < 16; ++t) {
    uint32_t kq[4];
#pragma unroll
    for (int j = 0; j < 4; ++j) { kq[j] = ka[j] + ring; PIN(kq[j]); }
    unsigned nx = t + 1;
    PIN(nx);
    ring = ring_next(ring);
    while (have <= t) {
      have = landed(ld_lds64(sya + S_FULL));
      if (have > t) break;
      __builtin_amdgcn_s_sleep(1);
    }
    CFENCE();
    SBAR();
    bf16x8 a[16];
#pragma unroll
    for (int i = 0; i < 16; ++i) { a[i] = *LP(const bf16x8, kq[(i >> 1) & 3] + 256 * (i >> 3) + 8192 * (i & 1)); SBAR(); }
    st_lds(fra, nx);
    SBAR();
    f32x4 n[2] = {zero, zero};
    u32x2 fv;
#pragma unroll
    for (int i = 0; i < 16; ++i) {
      n[i & 1] = MFMA(a[i], qf[i >> 1], n[i & 1]);
      SBAR();
      if (i == 12) fv = ld_lds64(sya + S_FULL);
      SBAR();
    }
    f32x4 e0, e1;
#pragma unroll
    for (int j = 0; j < 4; ++j) { e0[j] = __builtin_fmaf(n[0][j], SC, -m); PIN(e0[j]); }
#pragma unroll
    for (int j = 0; j < 4; ++j) { e1[j] = __builtin_fmaf(n[1][j], SC, -m); PIN(e1[j]); }
    if (__any(!(max8(e0, e1) <= SLACK))) {
      const float mn = vmax(m, __builtin_ceilf(xg_max(max8(n[0], n[1]) * SC)));
      const float df = fminf(mn - m, 255.f);
      m = mn;
      ls *= ex2(-df);
      const unsigned short d = (unsigned short)((unsigned)df << 7);
      pt = __builtin_bit_cast(u32x2, __builtin_elementwise_sub_sat(__builtin_bit_cast(u16x4, pt), u16x4{d, d, d, d}));
      const u16x8 dv = {d, d, d, d, d, d, d, d};
#pragma unroll 1
      for (int i = 0; i < t; ++i) {
        LAS u16x8* pp = LP(u16x8, pa + i * 1024);
        *pp = __builtin_elementwise_sub_sat(*pp, dv);
      }
#pragma unroll
      for (int j = 0; j < 4; ++j) { e0[j] = __builtin_fmaf(n[0][j], SC, -m); e1[j] = __builtin_fmaf(n[1][j], SC, -m); }
    }
    const u32x4 pf = {pk2(ex2(e0[0]), ex2(e0[1])), pk2(ex2(e0[2]), ex2(e0[3])), pk2(ex2(e1[0]), ex2(e1[1])), pk2(ex2(e1[2]), ex2(e1[3]))};
    *LP(u32x4, pw) = pf;
    pw += 1024;
    ls = MFMA(ones, __builtin_bit_cast(bf16x8, pf), ls);
    have = landed(fv);
  }
  f32x4 o[8];
  u32x4 e[6];
  u32x4 x0 = {0u, 0u, 0u, 0u}, x1 = x0, x2 = x0, x3 = x0;
  u32x2 st[3];
#pragma unroll
  for (int k = 0; k < 6; ++k) e[k] = u32x4{0u, 0u, 0u, 0u};
#pragma unroll
  for (int k = 0; k < 3; ++k) st[k] = u32x2{0u, 0u};
  bf16x4 tv[4];
  auto rtv = [&](int blk) {
#pragma unroll
    for (int db = 0; db < 4; ++db) tv[db] = __builtin_amdgcn_ds_read_tr16_b64_v4bf16(LP(bf16x4, tva ^ (32 * (4 * blk + db))));
    SBAR();
  };
  auto otree = [&](int hf) {
    SBAR();
#pragma unroll
    for (int db = 0; db < 4; ++db) o[4 * hf + db] = MFMA16(__builtin_bit_cast(s16x4, tv[db]), __builtin_bit_cast(s16x4, pt), zero);
  };
  auto pack = [&](const f32x4& a, const f32x4& b) { return u32x4{pk2(a[0], a[1]), pk2(a[2], a[3]), pk2(b[0], b[1]), pk2(b[2], b[3])}; };
  uint32_t vq[8];
  bf16x8 P[4];
  unsigned nx;
  int s = 16;
  auto prep = [&]() {
    const uint32_t pq = pa + (s < 24 ? (s & 7) * 2048 : (s & 3) * 4096), pq2 = pq + (s < 24 ? 0 : 2048);
#pragma unroll
    for (int i = 0; i < 4; ++i) P[i] = *LP(const bf16x8, (i < 2 ? pq : pq2) + (i & 1) * 1024);
#pragma unroll
    for (int j = 0; j < 4; ++j) { vq[j] = va[j] + ring; vq[j + 4] = vq[j] ^ 128; PIN(vq[j]); PIN(vq[j + 4]); }
    nx = s + 1;
    PIN(nx);
    ring = ring_next(ring);
    SBAR();
  };
  while (rfl(ld_lds(sya + S_TREE)) != 2) { }
  CFENCE();
  const int pb = ~split & 2;
  rtv(pb);
  otree(0);
  rtv(pb + 1);
  prep();
  otree(1);
  int gw = 0, ign = 1, left = 17;
  auto signal = [&]() {
    wait_vm<0>();
    u64 x = 0;
    if (lane == 0) x = __hip_atomic_fetch_add(LP(u64, sya + S_DRAIN), 1ull, __ATOMIC_RELAXED, __HIP_MEMORY_SCOPE_WORKGROUP);
    CFENCE();
    unsigned rv_ = ld_lds(sya + S_RV);
    if (rfl((unsigned)x) == 3) {
      unsigned cok = rfl((unsigned)(x >> 32));
      while (!cok) { __builtin_amdgcn_s_sleep(1); cok = rfl(ld_lds(sya + S_CHECKED)); CFENCE(); rv_ = ld_lds(sya + S_RV); }
      if (lane == 0) st_flag(flags + 4 + split, rv_);
    }
  };
  auto fetch = [&]() {
#pragma unroll
    for (int k = 0; k < 3; ++k) st[k] = __builtin_amdgcn_raw_buffer_load_b64(rs, mla, ((split + 3 - k) & 3) * 512, 16);
#pragma unroll
    for (int k = 0; k < 6; ++k) { SBAR(); e[k] = __builtin_amdgcn_raw_buffer_load_b128(rs, mbl + (k / 3) * 1024, split * 8192 + (k % 3) * MB_K, 16); }
    ign = 1;
    --left;
  };
#pragma unroll 1
  do {
    while (have <= s && gw <= ign) {
      const u32x4 x = ld_lds128(sya + S_FULL);
      have = landed(x);
      gw = rfl(x[2]);
      if (have > s) break;
      __builtin_amdgcn_s_sleep(1);
    }
    CFENCE();
    SPIN(gw);
    if (gw > ign) fetch();
    SPIN(have);
    SBAR();
    if (have > s) {
      bf16x4 lo[16], hi4[16];
      unsigned gv[2];
      auto rv = [&](int j) {
        lo[j] = __builtin_amdgcn_ds_read_tr16_b64_v4bf16(LP(bf16x4, vq[j & 7] + (j >> 3) * 8192));
        hi4[j] = __builtin_amdgcn_ds_read_tr16_b64_v4bf16(LP(bf16x4, vq[j & 7] + (j >> 3) * 8192 + 4096));
        if (j == 6 || j == 12) { SBAR(); gv[j == 12] = ld_lds(sya + S_GO); }
      };
#pragma unroll
      for (int j = 0; j < 7; ++j) { rv(j); SBAR(); }
      u32x4 fv;
#pragma unroll
      for (int j = 0; j < 16; ++j) {
        o[j & 7] = MFMA(__builtin_shufflevector(lo[j], hi4[j], 0, 1, 2, 3, 4, 5, 6, 7), P[((j >> 1) & 2) + (j >> 3)], o[j & 7]);
        SBAR();
        if (j < 9) rv(j + 7);
        if (j == 8) st_lds(fra, nx);
        if (j == 10) fv = ld_lds128(sya + S_FULL);
        if (j == 6 || j == 12) {
          int g2 = rfl(gv[j == 12]);
          SPIN(g2);
          CFENCE();
          if (g2 > ign) fetch();
        }
        SBAR();
      }
      have = landed(fv);
      gw = rfl(fv[2]);
      int ev = (0x1c800000u >> s) & 1;
      --left;
      ++s;
      SPIN(ev);
      if (ev) {
        int c = s;
        SPIN(c);
        auto put = [&](int blk, const u32x4& a, const u32x4& b) {
          const int so = blk * 8192 + ((blk - split - 1) & 3) * MB_K;
          __builtin_amdgcn_raw_buffer_store_b128(a, rs, mbl, so, 16);
          __builtin_amdgcn_raw_buffer_store_b128(b, rs, mbl + 1024, so, 16);
        };
        if ((c & 3) == 0) {
          rtv(split ^ ((~c >> 2) & 1));
          int d = c;
          SPIN(d);
          if (d == 24) {
            x0 = pack(o[0], o[1]); x1 = pack(o[2], o[3]); x2 = pack(o[4], o[5]); x3 = pack(o[6], o[7]);
          } else {
            x0 = pack(o[0] + o[4], o[1] + o[5]); x1 = pack(o[2] + o[6], o[3] + o[7]);
            put(split ^ 1, x0, x1);
          }
          otree(0);
#pragma unroll
          for (int db = 0; db < 4; ++db) o[4 + db] = zero;
        }
        SPIN(c);
        if (c == 27) {
          put(pb, x0, x1);
          if (g == 0) __builtin_amdgcn_raw_buffer_store_b64(u32x2{__float_as_uint(m), __float_as_uint(ls[0])}, rs, mla, split * 512, 16);
          put(pb + 1, x2, x3);
        }
        SPIN(c);
        if (c == 29) { signal(); ign = 0; }
      }
      prep();
    }
  } while (left);
  {
#pragma unroll
    for (int i = 0; i < 4; ++i) { o[i] += o[4 + i]; PIN(o[i]); }
    SBAR();
    float M = m;
#pragma unroll
    for (int k = 0; k < 3; ++k) M = vmax(M, __uint_as_float(st[k][0]));
    float w0 = ex2(m - M), wt[3];
#pragma unroll
    for (int k = 0; k < 3; ++k) wt[k] = ex2(__uint_as_float(st[k][0]) - M);
    const float inv = __builtin_amdgcn_rcpf((w0 * ls[0] + wt[0] * __uint_as_float(st[0][1])) + (wt[1] * __uint_as_float(st[1][1]) + wt[2] * __uint_as_float(st[2][1])));
    w0 *= inv; wt[0] *= inv; wt[1] *= inv; wt[2] *= inv;
#pragma unroll
    for (int c = 0; c < 2; ++c) {
      f32x4 v0 = o[2 * c] * w0, v1 = o[2 * c + 1] * w0;
#pragma unroll
      for (int k = 0; k < 3; ++k) {
        v0 += unpk(e[3 * c + k][0], e[3 * c + k][1]) * wt[k];
        v1 += unpk(e[3 * c + k][2], e[3 * c + k][3]) * wt[k];
      }
      const auto a = __builtin_amdgcn_permlane16_swap(pk2(v0[0], v0[1]), pk2(v1[0], v1[1]), false, false);
      const auto b = __builtin_amdgcn_permlane16_swap(pk2(v0[2], v0[3]), pk2(v1[2], v1[3]), false, false);
      __builtin_amdgcn_raw_buffer_store_b128(u32x4{a[0], b[0], a[1], b[1]}, ro, ooff + 64 * c, 0, 16);
      SBAR();
    }
  }
  return;
  }
  if (w == -1) {
    unsigned* sp = (unsigned*)(ws + WS_T0) + (lane & 7) * 32;
    if (lane == bid && lane < 8) st_flag(sp, (unsigned)t64);
    dma4(kt + troff, lds0 + TK_OFF, ko[0], ko[1], ko[2], ko[3]);
    bar();
    uint32_t vo[4];
#pragma unroll
    for (int j = 0; j < 4; ++j) vo[j] = hi * 512 + ((pos ^ ((2 * j + hi) << 1)) << 4);
    unsigned* fp = flags + (lane & 7);
    wait_vm<0>();
    st_lds(sya + S_TREE, 1u);
    dma4(vt + troff, lds0 + TV_OFF, vo[0], vo[1], vo[2], vo[3]);
    const unsigned f0 = ld_flag(fp);
    wait_vm<0>();
    st_lds(sya + S_TREE, 2u);
    if (lane == 4 + split) st_lds(sya + S_RV, f0 + 1u);
    unsigned beat = 0;
    auto upto = [&](int n) { while (landed(ld_lds64(sya + S_FULL)) <= n) { __builtin_amdgcn_s_sleep(8); } };
    auto bump = [&]() { ++beat; if (lane == split) st_flag(fp, f0 + beat); };
    auto changed = [&](int half, unsigned v) { return __all((lane & 3) == split || (lane & 4) != half || v != f0) != 0; };
    upto(2);
    {
      int sd = (int)(ld_flag(sp) - (unsigned)t64), sm = 0;
      sd = sd < -300 || sd > 300 ? 0 : sd;
#pragma unroll
      for (int i = 0; i < 8; ++i) sm += __builtin_amdgcn_readlane(sd, i);
      st_lds(sya + S_ADJ, (unsigned)((sm * 85) >> 10));
    }
    upto(6);
    for (unsigned it = 0; ; ++it) {
      if ((it & 15) == 0) bump();
      __builtin_amdgcn_s_sleep(32);
      if (changed(0, ld_flag(fp))) break;
    }
    st_lds(sya + S_CHECKED, 1u);
    upto(30);
    for (unsigned it = 1; !changed(4, ld_flag(fp)); ++it) {
      if ((it & 15) == 0) bump();
    }
    st_lds(sya + S_GO, 1u);
    return;
  }
  {
    const uint32_t ld0 = lds0 + h * 8192;
    dma4(kb + SLOT, ld0 + SLOT, ko[0], ko[1], ko[2], ko[3]);
    dma4(kb + SLOT, ld0 + SLOT + 4096, ko[4], ko[5], ko[6], ko[7]);
    const char* vb = vc + kvoff + h * 16384;
    uint32_t fa = sya + S_FREE + (lane & 3) * 4, fu = sya + S_FULL + 4 * h;
    PIN(fa); PIN(fu);
    uint32_t dst = 2 * SLOT;
    SPIN(t64);
    const unsigned tstart = (unsigned)t64;
    int due = T_OFF + 2 * T_K;
    int adj;
    auto early = [&]() { return (int)((((unsigned)__builtin_amdgcn_s_memrealtime() - tstart) & 0xffffffu) << 4) < due + adj; };
    bar();
#pragma unroll 1
    for (int n = 2; n < 32; ++n) {
      int fw = (int)ld_lds(fa);
      adj = rfl(ld_lds(sya + S_ADJ)) * min(max(n - 4, 0), 24);
      if ((n & 7) == 0 && n >= 16) {
#pragma unroll
        for (int j = 0; j < 8; ++j) {
          const int c = (lane & 15) ^ (((4 * j + (lane >> 4)) & 7) << 1);
          ko[j] = (lane >> 4) * 512 + (c & 7) * 16 + (c >> 3) * (n == 16 ? 128 : 32768) + j * 2048 - (j & 3) * 1024;
        }
      }
      const char* src = n < 16 ? kb + (size_t)n * SLOT : n < 24 ? vb + (size_t)(n & 7) * 32768 + (~split & 2) * 128 : vb + (size_t)(n & 3) * 65536 + (split ^ ((~n >> 2) & 1)) * 128;
      uint32_t d0 = ld0 + dst, d1 = d0 + 4096;
      dst = ring_next(dst);
      asm volatile("" : "+s"(src), "+s"(d0), "+s"(d1), "+s"(dst));
      unsigned fl = n - 1;
      PIN(fl);
      const int need = n >= 31 ? 28 : n - NRING;
      const bool busy = __any(fw <= need);
      SBAR();
      wait_vm<16>();
      st_lds(fu, fl - 1);
      if (n == 31 || (n >= 3 && early())) {
        wait_vm<8>();
        st_lds(fu, fl);
      }
      if (busy) {
        do { fw = (int)ld_lds(fa); } while (__any(fw <= need));
      }
      while (early()) __builtin_amdgcn_s_sleep(1);
      due += n == 29 ? T_V1 + T_HOLE : n < 16 ? T_K : n < 24 ? T_V2 : T_V1;
      dma4(src, d0, ko[0], ko[1], ko[2], ko[3]);
      dma4(src, d1, ko[4], ko[5], ko[6], ko[7]);
    }
    wait_vm<16>();  st_lds(fu, 30u);
    wait_vm<8>();  st_lds(fu, 31u);
    wait_vm<0>();  st_lds(fu, 32u);
  }
}
}

extern "C" void kernel_launch(void* const* d_in, const int* in_sizes, int n_in, void* d_out, int out_size, void* d_ws,
                              size_t ws_size, hipStream_t stream) {
  (void)hipFuncSetAttribute((const void*)attn_tree, hipFuncAttributeMaxDynamicSharedMemorySize, LDS_BYTES);
  attn_tree<<<NGROUP * NSPLIT, 448, LDS_BYTES, stream>>>((const char*)d_in[0], (const char*)d_in[1], (const char*)d_in[2],
                                                 (const char*)d_in[3], (const char*)d_in[4], (const char*)d_in[5],
                                                 (char*)d_ws, (unsigned short*)d_out);
}
```

```cpp
#include <hip/hip_runtime.h>
#include <hip/hip_bf16.h>
#include <stdint.h>

namespace {
constexpr int NB = 8, HKV = 8, CC = 2048, DD = 256;
constexpr int NSPLIT = 4;
constexpr int NGROUP = NB * HKV;
constexpr int SLOT = 16384;
constexpr int NRING = 5;
constexpr int TK_OFF = NRING * SLOT;
constexpr int TV_OFF = TK_OFF + 4096;
constexpr int P_OFF = TV_OFF + 4096;
constexpr int PW = 16 * 1024;
constexpr int SY_OFF = P_OFF + 4 * PW;
constexpr int S_FREE = 64;
constexpr int S_PD = 0;
constexpr int S_DRAIN = 16, S_CHECKED = 20;
constexpr int S_RV = 24;
constexpr int S_FULL = 32;
constexpr int S_GO = 40, S_TREE = 44;
constexpr int S_ADJ = 48;
constexpr int LDS_BYTES = SY_OFF + 128;
constexpr float LOG2E = 1.4426950408889634f;
constexpr float SC = 0.0625f * LOG2E;
constexpr float MASKR = -1.0e9f * 16.0f;
constexpr float SLACK = 10.0f;
constexpr int T_OFF = 45 * 16, T_K = 885, T_V2 = 884, T_V1 = 900, T_HOLE = 100 * 16;
constexpr int WS_FLAG = 0;
constexpr int WS_T0 = 16384;
constexpr int WS_MB = 65536;
constexpr int MB_K = 2 << 20;
constexpr int WS_ML = WS_MB + 3 * MB_K;
constexpr int WS_END = WS_ML + 64 * 2048;

typedef __attribute__((ext_vector_type(8))) __bf16 bf16x8;
typedef __attribute__((ext_vector_type(4))) __bf16 bf16x4;
typedef __attribute__((ext_vector_type(4))) short s16x4;
typedef __attribute__((ext_vector_type(4))) unsigned short u16x4;
typedef __attribute__((ext_vector_type(8))) unsigned short u16x8;
typedef __attribute__((ext_vector_type(4))) float f32x4;
typedef __attribute__((ext_vector_type(4))) unsigned u32x4;
typedef __attribute__((ext_vector_type(2))) unsigned u32x2;
typedef unsigned long long u64;
#define LAS __attribute__((address_space(3)))
#define GAS __attribute__((address_space(1)))
#define DI __device__ __forceinline__
#define SBAR() __builtin_amdgcn_sched_barrier(0)
#define PIN(x) asm volatile("" : "+v"(x))
#define SPIN(x) asm volatile("" : "+s"(x))
#define CFENCE() asm volatile("" ::: "memory")
#define LP(T, a) ((LAS T*)(uintptr_t)(a))
#define MFMA(a, b, c) __builtin_amdgcn_mfma_f32_16x16x32_bf16(a, b, c, 0, 0, 0)
#define MFMA16(a, b, c) __builtin_amdgcn_mfma_f32_16x16x16bf16_1k(a, b, c, 0, 0, 0)

DI void dma4(const char* sbase, uint32_t m0v, uint32_t v0, uint32_t v1, uint32_t v2, uint32_t v3) {
  uint32_t keep;
  asm volatile(
      "s_mov_b32 %0, m0\n\ts_mov_b32 m0, %6\n\ts_nop 0\n\t"
      "global_load_lds_dwordx4 %1, %5 nt\n\t"
      "global_load_lds_dwordx4 %2, %5 offset:1024 nt\n\t"
      "global_load_lds_dwordx4 %3, %5 offset:2048 nt\n\t"
      "global_load_lds_dwordx4 %4, %5 offset:3072 nt\n\t"
      "s_mov_b32 m0, %0"
      : "=&s"(keep) : "v"(v0), "v"(v1), "v"(v2), "v"(v3), "s"(sbase), "s"(m0v) : "memory");
}
template <int N> DI void wait_vm() { asm volatile("s_waitcnt vmcnt(%0)" ::"i"(N) : "memory"); }
DI void bar() { asm volatile("s_waitcnt lgkmcnt(0)\n\ts_barrier" ::: "memory"); }
DI float vmax(float a, float b) { return __builtin_elementwise_maximum(a, b); }
DI float ex2(float x) { return __builtin_amdgcn_exp2f(x); }
DI float xg_max(float x) {
  auto s = __builtin_amdgcn_permlane16_swap(__float_as_uint(x), __float_as_uint(x), false, false);
  x = vmax(__uint_as_float(s[0]), __uint_as_float(s[1]));
  auto t = __builtin_amdgcn_permlane32_swap(__float_as_uint(x), __float_as_uint(x), false, false);
  return vmax(__uint_as_float(t[0]), __uint_as_float(t[1]));
}
DI float max8(const f32x4& a, const f32x4& b) { return vmax(vmax(vmax(vmax(a[0], a[1]), a[2]), vmax(vmax(a[3], b[0]), b[1])), vmax(b[2], b[3])); }
DI unsigned ld_flag(const unsigned* p) { return __hip_atomic_load((const GAS unsigned*)p, __ATOMIC_RELAXED, __HIP_MEMORY_SCOPE_AGENT); }
DI void st_flag(unsigned* p, unsigned v) { __hip_atomic_store((GAS unsigned*)p, v, __ATOMIC_RELAXED, __HIP_MEMORY_SCOPE_AGENT); }
DI unsigned ld_lds(uint32_t a) { return __hip_atomic_load(LP(unsigned, a), __ATOMIC_RELAXED, __HIP_MEMORY_SCOPE_WORKGROUP); }
DI u32x4 ld_lds128(uint32_t a) { return *LP(volatile u32x4, a); }
DI void st_lds(uint32_t a, unsigned v) { __hip_atomic_store(LP(unsigned, a), v, __ATOMIC_RELAXED, __HIP_MEMORY_SCOPE_WORKGROUP); }
DI int rfl(unsigned x) { return __builtin_amdgcn_readfirstlane((int)x); }
DI u32x2 ld_lds64(uint32_t a) { return *LP(volatile u32x2, a); }
template <class T> DI int landed(const T& x) { return rfl(x[0] < x[1] ? x[0] : x[1]); }
DI unsigned pk2(float a, float b) { typedef __attribute__((ext_vector_type(2))) __bf16 bf16x2; const bf16x2 v = {(__bf16)a, (__bf16)b}; return __builtin_bit_cast(unsigned, v); }
DI f32x4 unpk(unsigned x0, unsigned x1) { return f32x4{__uint_as_float(x0 << 16), __uint_as_float(x0 & 0xffff0000u), __uint_as_float(x1 << 16), __uint_as_float(x1 & 0xffff0000u)}; }
DI uint32_t ring_next(uint32_t x) { return x == (NRING - 1) * SLOT ? 0 : x + SLOT; }

__global__ void __launch_bounds__(704)
attn_tree(const char* __restrict__ q, const char* __restrict__ kc, const char* __restrict__ vc,
          const char* __restrict__ kt, const char* __restrict__ vt, const char* __restrict__ mask,
          char* __restrict__ ws, unsigned short* __restrict__ out) {
  extern __shared__ __attribute__((aligned(1024))) char smem_[];
  const int tid = threadIdx.x, lane = tid & 63;
  const int wi = rfl(tid >> 6);
  const int w = wi < 7 ? wi - 3 : wi - 7, c = wi >= 7;
  const int bid = blockIdx.x;
  const int grp = (bid & 7) * 8 + (bid >> 5), split = (bid >> 3) & 3;
  const uint32_t lds0 = (uint32_t)(uintptr_t)(LAS char*)smem_;
  const int hi = lane >> 5, pos = lane & 31;
  const size_t kvoff = ((size_t)grp * CC + (size_t)split * (CC / NSPLIT)) * 512;
  const size_t troff = ((size_t)grp * 32 + 8 * split) * 512;
  uint32_t ko[8];
#pragma unroll
  for (int j = 0; j < 8; ++j) { ko[j] = (j >> 2) * 4096 + hi * 512 + ((pos ^ (2 * j + hi)) << 4); PIN(ko[j]); }
  uint32_t sya = lds0 + SY_OFF;
  PIN(sya);
  st_lds(sya + (lane & 31) * 4, 0u);
  asm volatile("" ::"s"(q), "s"(kc), "s"(vc), "s"(kt), "s"(vt), "s"(mask), "s"(ws), "s"(out));
  u64 t64 = __builtin_amdgcn_s_memrealtime();
  const int h = ~w & 1;
  const char* kb = kc + kvoff + h * 8192;
  if (w != -1 && !c) {
    const char* src = w >= 0 ? q + ((size_t)grp * 64 + 16 * w) * 512 : kb;
    const uint32_t dst = lds0 + (w >= 0 ? P_OFF + w * PW : h * 8192);
    dma4(src, dst, ko[0], ko[1], ko[2], ko[3]);
    dma4(src, dst + 4096, ko[4], ko[5], ko[6], ko[7]);
  }
  unsigned* flags = (unsigned*)(ws + WS_FLAG) + (size_t)grp * 32;
  if (w >= 0) {
  const int r = lane & 15, g = lane >> 4;
  bar();
  u32x2 mk = {0u, 0u};
  if (!c) mk = *(const u32x2*)(mask + (((grp >> 3) * 32 + 16 * (w & 1) + r) * 32 + 8 * split + 4 * (g & 1)) * 2);
  SBAR();
  const __amdgpu_buffer_rsrc_t rs = __builtin_amdgcn_make_buffer_rsrc(ws, 0, WS_END, 0x00020000);
  uint32_t ka[4], va[4];
  const int vrow = 4 * g + (r >> 2);
#pragma unroll
  for (int j = 0; j < 4; ++j) {
    ka[j] = lds0 + ((r * 512 + ((g ^ r) << 4)) ^ (64 * j));
    va[j] = lds0 + ((vrow * 256 + ((vrow & 7) << 5) + ((r & 3) << 3)) ^ (32 * j + 128 * c));
    PIN(ka[j]); PIN(va[j]);
  }
  uint32_t pa = lds0 + P_OFF + w * PW + lane * 16;
  uint32_t tva = lds0 + TV_OFF + (vrow & 7) * 512 + ((vrow & 7) << 5) + ((r & 3) << 3);
  uint32_t mbl = WS_MB + grp * 32768 + w * 2048 + lane * 16;
  uint32_t mla = WS_ML + grp * 2048 + (16 * w + r) * 8;
  const __amdgpu_buffer_rsrc_t ro = __builtin_amdgcn_make_buffer_rsrc(out, 0, NGROUP * 64 * DD * 2, 0x00020000);
  uint32_t ooff = (uint32_t)(((grp * 64 + 16 * w + r) * DD + 64 * split + 32 * c + 16 * (g & 1) + 8 * (g >> 1)) * 2);
  uint32_t tka = lds0 + TK_OFF + (r & 7) * 512 + ((g ^ (r & 7)) << 4);
  uint32_t fra = sya + S_FREE + 4 * (wi - 3);
  uint32_t xa = lds0 + TK_OFF + w * 1024 + lane * 16;
  uint32_t mbc = mbl + 1024 * c;
  PIN(pa); PIN(tva); PIN(mbl); PIN(mla); PIN(ooff); PIN(tka); PIN(fra); PIN(xa); PIN(mbc);
  const f32x4 zero = {0.f, 0.f, 0.f, 0.f};
  float m, l;
  u32x2 pt;
  int have;
  if (c) {
    st_lds(fra, 16u);
    while (landed(ld_lds64(sya + S_FULL)) < 15) __builtin_amdgcn_s_sleep(16);
    while (!rfl(ld_lds(sya + S_PD + 4 * w))) __builtin_amdgcn_s_sleep(1);
    CFENCE();
    const u32x4 x = ld_lds128(xa);
    pt = u32x2{x[0], x[1]};
    m = __uint_as_float(x[2]);
    l = __uint_as_float(x[3]);
    have = 0;
  } else {
  const __bf16 one = (__bf16)1.0f;
  bf16x8 ones = {one, one, one, one, one, one, one, one};
  PIN(ones);
  wait_vm<1>();
  bf16x8 qf[8];
#pragma unroll
  for (int s = 0; s < 8; ++s) qf[s] = *LP(const bf16x8, ka[s & 3] + (P_OFF + w * PW) + 256 * (s >> 2));
  for (;;) {
    const u32x4 x = ld_lds128(sya + S_FULL);
    have = landed(x);
    if (rfl(x[3])) break;
    __builtin_amdgcn_s_sleep(1);
  }
  CFENCE();
  f32x4 ls;
  {
    bf16x8 a[8];
#pragma unroll
    for (int s = 0; s < 8; ++s) a[s] = *LP(const bf16x8, (tka ^ (64 * (s & 3))) + 256 * (s >> 2));
    f32x4 n = zero;
#pragma unroll
    for (int s = 0; s < 8; ++s) n = MFMA(a[s], qf[s], n);
    const f32x4 mv = unpk(mk[0], mk[1]);
#pragma unroll
    for (int j = 0; j < 4; ++j) n[j] = g < 2 ? n[j] + (1.0f - mv[j]) * MASKR : -__builtin_inff();
    m = vmax(-3.0e38f, __builtin_ceilf(xg_max(vmax(vmax(n[0], n[1]), vmax(n[2], n[3])) * SC)));
    pt = u32x2{pk2(ex2(__builtin_fmaf(n[0], SC, -m)), ex2(__builtin_fmaf(n[1], SC, -m))), pk2(ex2(__builtin_fmaf(n[2], SC, -m)), ex2(__builtin_fmaf(n[3], SC, -m)))};
    ls = MFMA(ones, __builtin_bit_cast(bf16x8, u32x4{pt[0], pt[1], 0u, 0u}), zero);
  }
  uint32_t ring = 0;
  uint32_t pw = pa;
#pragma unroll 1
  for (int t = 0; t < 16; ++t) {
    uint32_t kq[4];
#pragma unroll
    for (int j = 0; j < 4; ++j) { kq[j] = ka[j] + ring; PIN(kq[j]); }
    unsigned nx = t + 1;
    PIN(nx);
    ring = ring_next(ring);
    while (have <= t) {
      have = landed(ld_lds64(sya + S_FULL));
      if (have > t) break;
      __builtin_amdgcn_s_sleep(1);
    }
    CFENCE();
    SBAR();
    bf16x8 a[16];
#pragma unroll
    for (int i = 0; i < 16; ++i) { a[i] = *LP(const bf16x8, kq[(i >> 1) & 3] + 256 * (i >> 3) + 8192 * (i & 1)); SBAR(); }
    st_lds(fra, nx);
    SBAR();
    f32x4 n[2] = {zero, zero};
    u32x2 fv;
#pragma unroll
    for (int i = 0; i < 16; ++i) {
      n[i & 1] = MFMA(a[i], qf[i >> 1], n[i & 1]);
      SBAR();
      if (i == 12) fv = ld_lds64(sya + S_FULL);
      SBAR();
    }
    f32x4 e0, e1;
#pragma unroll
    for (int j = 0; j < 4; ++j) { e0[j] = __builtin_fmaf(n[0][j], SC, -m); PIN(e0[j]); }
#pragma unroll
    for (int j = 0; j < 4; ++j) { e1[j] = __builtin_fmaf(n[1][j], SC, -m); PIN(e1[j]); }
    if (__any(!(max8(e0, e1) <= SLACK))) {
      const float mn = vmax(m, __builtin_ceilf(xg_max(max8(n[0], n[1]) * SC)));
      const float df = fminf(mn - m, 255.f);
      m = mn;
      ls *= ex2(-df);
      const unsigned short d = (unsigned short)((unsigned)df << 7);
      pt = __builtin_bit_cast(u32x2, __builtin_elementwise_sub_sat(__builtin_bit_cast(u16x4, pt), u16x4{d, d, d, d}));
      const u16x8 dv = {d, d, d, d, d, d, d, d};
#pragma unroll 1
      for (int i = 0; i < t; ++i) {
        LAS u16x8* pp = LP(u16x8, pa + i * 1024);
        *pp = __builtin_elementwise_sub_sat(*pp, dv);
      }
#pragma unroll
      for (int j = 0; j < 4; ++j) { e0[j] = __builtin_fmaf(n[0][j], SC, -m); e1[j] = __builtin_fmaf(n[1][j], SC, -m); }
    }
    const u32x4 pf = {pk2(ex2(e0[0]), ex2(e0[1])), pk2(ex2(e0[2]), ex2(e0[3])), pk2(ex2(e1[0]), ex2(e1[1])), pk2(ex2(e1[2]), ex2(e1[3]))};
    *LP(u32x4, pw) = pf;
    pw += 1024;
    ls = MFMA(ones, __builtin_bit_cast(bf16x8, pf), ls);
    have = landed(fv);
  }
  l = ls[0];
  *LP(u32x4, xa) = u32x4{pt[0], pt[1], __float_as_uint(m), __float_as_uint(l)};
  CFENCE();
  st_lds(sya + S_PD + 4 * w, 1u);
  }
  uint32_t ring = SLOT;
  f32x4 o0, o1, o2, o3;
  uint32_t v0 = va[0], v1 = va[1], v2 = va[2], v3 = va[3];
  u32x4 e[3];
  u32x4 x0 = {0u, 0u, 0u, 0u}, x1 = x0;
  u32x2 st[3];
#pragma unroll
  for (int k = 0; k < 3; ++k) { e[k] = u32x4{0u, 0u, 0u, 0u}; st[k] = u32x2{0u, 0u}; }
  auto rtv = [&](int i) { return __builtin_bit_cast(s16x4, __builtin_amdgcn_ds_read_tr16_b64_v4bf16(LP(bf16x4, tva ^ (32 * i)))); };
  auto pack = [&](const f32x4& a, const f32x4& b) { return u32x4{pk2(a[0], a[1]), pk2(a[2], a[3]), pk2(b[0], b[1]), pk2(b[2], b[3])}; };
  uint32_t vq[4];
  bf16x8 P[4];
  unsigned nx;
  int s = 16;
  auto prep = [&]() {
    const uint32_t pq = pa + (s < 24 ? (s & 7) * 2048 : (s & 3) * 4096), pq2 = pq + (s < 24 ? 0 : 2048);
#pragma unroll
    for (int i = 0; i < 4; ++i) P[i] = *LP(const bf16x8, (i < 2 ? pq : pq2) + (i & 1) * 1024);
#pragma unroll
    for (int j = 0; j < 4; ++j) { vq[j] = (j == 0 ? v0 : j == 1 ? v1 : j == 2 ? v2 : v3) + ring; PIN(vq[j]); }
    nx = s + 1;
    PIN(nx);
    ring = ring_next(ring);
    SBAR();
  };
  while (rfl(ld_lds(sya + S_TREE)) != 2) { }
  CFENCE();
  const int pb = (~split & 2) + c;
  {
    s16x4 tv[4];
#pragma unroll
    for (int db = 0; db < 4; ++db) tv[db] = rtv(4 * pb + db);
    SBAR();
    prep();
    o0 = MFMA16(tv[0], __builtin_bit_cast(s16x4, pt), zero);
    o1 = MFMA16(tv[1], __builtin_bit_cast(s16x4, pt), zero);
    o2 = MFMA16(tv[2], __builtin_bit_cast(s16x4, pt), zero);
    o3 = MFMA16(tv[3], __builtin_bit_cast(s16x4, pt), zero);
  }
  int gw = 0, ign = 1, left = 17;
  auto signal = [&]() {
    wait_vm<0>();
    u64 x = 0;
    if (lane == 0) x = __hip_atomic_fetch_add(LP(u64, sya + S_DRAIN), 1ull, __ATOMIC_RELAXED, __HIP_MEMORY_SCOPE_WORKGROUP);
    CFENCE();
    unsigned rv_ = ld_lds(sya + S_RV);
    if (rfl((unsigned)x) == 7) {
      unsigned cok = rfl((unsigned)(x >> 32));
      while (!cok) { __builtin_amdgcn_s_sleep(1); cok = rfl(ld_lds(sya + S_CHECKED)); CFENCE(); rv_ = ld_lds(sya + S_RV); }
      if (lane == 0) st_flag(flags + 4 + split, rv_);
    }
  };
  auto fetch = [&]() {
#pragma unroll
    for (int k = 0; k < 3; ++k) st[k] = __builtin_amdgcn_raw_buffer_load_b64(rs, mla, ((split + 3 - k) & 3) * 512, 16);
#pragma unroll
    for (int k = 0; k < 3; ++k) { SBAR(); e[k] = __builtin_amdgcn_raw_buffer_load_b128(rs, mbc, split * 8192 + k * MB_K, 16); }
    ign = 1;
    --left;
  };
#pragma unroll 1
  do {
    while (have <= s && gw <= ign) {
      const u32x4 x = ld_lds128(sya + S_FULL);
      have = landed(x);
      gw = rfl(x[2]);
      if (have > s) break;
      __builtin_amdgcn_s_sleep(1);
    }
    CFENCE();
    SPIN(gw);
    if (gw > ign) fetch();
    SPIN(have);
    SBAR();
    if (have > s) {
      bf16x4 lo[8], hi4[8];
      unsigned gv;
      auto rv = [&](int j) {
        lo[j] = __builtin_amdgcn_ds_read_tr16_b64_v4bf16(LP(bf16x4, vq[j & 3] + (j >> 2) * 8192));
        hi4[j] = __builtin_amdgcn_ds_read_tr16_b64_v4bf16(LP(bf16x4, vq[j & 3] + (j >> 2) * 8192 + 4096));
        if (j == 3) { SBAR(); gv = ld_lds(sya + S_GO); }
      };
#pragma unroll
      for (int j = 0; j < 7; ++j) { rv(j); SBAR(); }
      u32x4 fv;
#pragma unroll
      for (int j = 0; j < 8; ++j) {
        f32x4& o = (j & 3) == 0 ? o0 : (j & 3) == 1 ? o1 : (j & 3) == 2 ? o2 : o3;
        o = MFMA(__builtin_shufflevector(lo[j], hi4[j], 0, 1, 2, 3, 4, 5, 6, 7), P[(j & 2) + (j >> 2)], o);
        SBAR();
        if (j == 0) { rv(7); st_lds(fra, nx); }
        if (j == 3) fv = ld_lds128(sya + S_FULL);
        if (j == 3) {
          int g2 = rfl(gv);
          SPIN(g2);
          CFENCE();
          if (g2 > ign) fetch();
        }
        SBAR();
      }
      have = landed(fv);
      gw = rfl(fv[2]);
      int ev = (0x1c800000u >> s) & 1;
      --left;
      ++s;
      SPIN(ev);
      if (ev) {
        int cs = s;
        SPIN(cs);
        auto so = [&](int blk) { return blk * 8192 + ((blk - split - 1) & 3) * MB_K; };
        if ((cs & 3) == 0) {
          const int tb = 4 * (split ^ ((~cs >> 2) & 1)) + 2 * c;
          const s16x4 t0 = rtv(tb), t1 = rtv(tb + 1);
          SBAR();
          int d = cs;
          SPIN(d);
          if (d == 24) {
            x0 = pack(o0, o1); x1 = pack(o2, o3);
            v0 ^= 192 * c; v1 = v0 ^ 32; v2 = v0 ^ 128; v3 = v0 ^ 160;
          } else {
            x0 = pack(o0 + o2, o1 + o3);
            __builtin_amdgcn_raw_buffer_store_b128(x0, rs, mbc, so(split ^ 1), 16);
          }
          SBAR();
          o0 = MFMA16(t0, __builtin_bit_cast(s16x4, pt), zero);
          o1 = MFMA16(t1, __builtin_bit_cast(s16x4, pt), zero);
          o2 = zero;
          o3 = zero;
        }
        SPIN(cs);
        if (cs == 27) {
          __builtin_amdgcn_raw_buffer_store_b128(x0, rs, mbl, so(pb), 16);
          __builtin_amdgcn_raw_buffer_store_b128(x1, rs, mbl + 1024, so(pb), 16);
          if (g == 4 * c) __builtin_amdgcn_raw_buffer_store_b64(u32x2{__float_as_uint(m), __float_as_uint(l)}, rs, mla, split * 512, 16);
        }
        SPIN(cs);
        if (cs == 29) { signal(); ign = 0; }
      }
      prep();
    }
  } while (left);
  {
    o0 += o2; o1 += o3;
    PIN(o0); PIN(o1);
    SBAR();
    float M = m;
#pragma unroll
    for (int k = 0; k < 3; ++k) M = vmax(M, __uint_as_float(st[k][0]));
    float w0 = ex2(m - M), wt[3];
#pragma unroll
    for (int k = 0; k < 3; ++k) wt[k] = ex2(__uint_as_float(st[k][0]) - M);
    const float inv = __builtin_amdgcn_rcpf((w0 * l + wt[0] * __uint_as_float(st[0][1])) + (wt[1] * __uint_as_float(st[1][1]) + wt[2] * __uint_as_float(st[2][1])));
    w0 *= inv; wt[0] *= inv; wt[1] *= inv; wt[2] *= inv;
    f32x4 y0 = o0 * w0, y1 = o1 * w0;
#pragma unroll
    for (int k = 0; k < 3; ++k) {
      y0 += unpk(e[k][0], e[k][1]) * wt[k];
      y1 += unpk(e[k][2], e[k][3]) * wt[k];
    }
    const auto a = __builtin_amdgcn_permlane16_swap(pk2(y0[0], y0[1]), pk2(y1[0], y1[1]), false, false);
    const auto b = __builtin_amdgcn_permlane16_swap(pk2(y0[2], y0[3]), pk2(y1[2], y1[3]), false, false);
    __builtin_amdgcn_raw_buffer_store_b128(u32x4{a[0], b[0], a[1], b[1]}, ro, ooff, 0, 16);
  }
  return;
  }
  if (w == -1) {
    unsigned* sp = (unsigned*)(ws + WS_T0) + (lane & 7) * 32;
    if (lane == bid && lane < 8) st_flag(sp, (unsigned)t64);
    bar();
    dma4(kt + troff, lds0 + TK_OFF, ko[0], ko[1], ko[2], ko[3]);
    uint32_t vo[4];
#pragma unroll
    for (int j = 0; j < 4; ++j) vo[j] = hi * 512 + ((pos ^ ((2 * j + hi) << 1)) << 4);
    unsigned* fp = flags + (lane & 7);
    wait_vm<0>();
    st_lds(sya + S_TREE, 1u);
    dma4(vt + troff, lds0 + TV_OFF, vo[0], vo[1], vo[2], vo[3]);
    const unsigned f0 = ld_flag(fp);
    wait_vm<0>();
    st_lds(sya + S_TREE, 2u);
    if (lane == 4 + split) st_lds(sya + S_RV, f0 + 1u);
    unsigned beat = 0;
    auto upto = [&](int n) { while (landed(ld_lds64(sya + S_FULL)) <= n) { __builtin_amdgcn_s_sleep(8); } };
    auto bump = [&]() { ++beat; if (lane == split) st_flag(fp, f0 + beat); };
    auto changed = [&](int half, unsigned v) { return __all((lane & 3) == split || (lane & 4) != half || v != f0) != 0; };
    upto(2);
    {
      int sd = (int)(ld_flag(sp) - (unsigned)t64), sm = 0;
      sd = sd < -300 || sd > 300 ? 0 : sd;
#pragma unroll
      for (int i = 0; i < 8; ++i) sm += __builtin_amdgcn_readlane(sd, i);
      st_lds(sya + S_ADJ, (unsigned)((sm * 85) >> 10));
    }
    upto(6);
    for (unsigned it = 0; ; ++it) {
      if ((it & 15) == 0) bump();
      __builtin_amdgcn_s_sleep(32);
      if (changed(0, ld_flag(fp))) break;
    }
    st_lds(sya + S_CHECKED, 1u);
    upto(30);
    for (unsigned it = 1; !changed(4, ld_flag(fp)); ++it) {
      if ((it & 15) == 0) bump();
    }
    st_lds(sya + S_GO, 1u);
    return;
  }
  {
    const uint32_t ld0 = lds0 + h * 8192;
    dma4(kb + SLOT, ld0 + SLOT, ko[0], ko[1], ko[2], ko[3]);
    dma4(kb + SLOT, ld0 + SLOT + 4096, ko[4], ko[5], ko[6], ko[7]);
    const char* vb = vc + kvoff + h * 16384;
    uint32_t fa = sya + S_FREE + (lane & 7) * 4, fu = sya + S_FULL + 4 * h;
    PIN(fa); PIN(fu);
    uint32_t dst = 2 * SLOT;
    SPIN(t64);
    const unsigned tstart = (unsigned)t64;
    int due = T_OFF + 2 * T_K;
    int adj;
    auto early = [&]() { return (int)((((unsigned)__builtin_amdgcn_s_memrealtime() - tstart) & 0xffffffu) << 4) < due + adj; };
    bar();
#pragma unroll 1
    for (int n = 2; n < 32; ++n) {
      int fw = (int)ld_lds(fa);
      adj = rfl(ld_lds(sya + S_ADJ)) * min(max(n - 4, 0), 24);
      if ((n & 7) == 0 && n >= 16) {
#pragma unroll
        for (int j = 0; j < 8; ++j) {
          const int c = (lane & 15) ^ (((4 * j + (lane >> 4)) & 7) << 1);
          ko[j] = (lane >> 4) * 512 + (c & 7) * 16 + (c >> 3) * (n == 16 ? 128 : 32768) + j * 2048 - (j & 3) * 1024;
        }
      }
      const char* src = n < 16 ? kb + (size_t)n * SLOT : n < 24 ? vb + (size_t)(n & 7) * 32768 + (~split & 2) * 128 : vb + (size_t)(n & 3) * 65536 + (split ^ ((~n >> 2) & 1)) * 128;
      uint32_t d0 = ld0 + dst, d1 = d0 + 4096;
      dst = ring_next(dst);
      asm volatile("" : "+s"(src), "+s"(d0), "+s"(d1), "+s"(dst));
      unsigned fl = n - 1;
      PIN(fl);
      const int need = n >= 31 ? 28 : n - NRING;
      const bool busy = __any(fw <= need);
      SBAR();
      wait_vm<16>();
      st_lds(fu, fl - 1);
      if (n < 3 || n == 31 || early()) {
        wait_vm<8>();
        st_lds(fu, fl);
      }
      if (busy) {
        do { fw = (int)ld_lds(fa); } while (__any(fw <= need));
      }
      while (early()) __builtin_amdgcn_s_sleep(1);
      due += n == 29 ? T_V1 + T_HOLE : n < 16 ? T_K : n < 24 ? T_V2 : T_V1;
      dma4(src, d0, ko[0], ko[1], ko[2], ko[3]);
      dma4(src, d1, ko[4], ko[5], ko[6], ko[7]);
    }
    wait_vm<16>();  st_lds(fu, 30u);
    wait_vm<8>();  st_lds(fu, 31u);
    wait_vm<0>();  st_lds(fu, 32u);
  }
}
}

extern "C" void kernel_launch(void* const* d_in, const int* in_sizes, int n_in, void* d_out, int out_size, void* d_ws,
                              size_t ws_size, hipStream_t stream) {
  (void)hipFuncSetAttribute((const void*)attn_tree, hipFuncAttributeMaxDynamicSharedMemorySize, LDS_BYTES);
  attn_tree<<<NGROUP * NSPLIT, 704, LDS_BYTES, stream>>>((const char*)d_in[0], (const char*)d_in[1], (const char*)d_in[2],
                                                 (const char*)d_in[3], (const char*)d_in[4], (const char*)d_in[5],
                                                 (char*)d_ws, (unsigned short*)d_out);
}
```
